```python
import torch

def _softmax(x, axis=-1):
    return torch.softmax(x, dim=axis)
def _gelu(x):
    return torch.nn.functional.gelu(x, approximate="tanh")
def _silu(x):
    return torch.nn.functional.silu(x)
def _sigmoid(x):
    return torch.sigmoid(x)
def _layernorm(x, axis=-1, eps=1e-5):
    m = x.mean(dim=axis, keepdim=True)
    v = x.var(dim=axis, keepdim=True, unbiased=False)
    return (x - m) / torch.sqrt(v + eps)
def _scatter_add(tgt, idx, src, axis=0):
    return tgt.clone().index_add_(axis, idx, src)
def _segment_sum(src, seg, n):
    out = torch.zeros((n,) + src.shape[1:], dtype=src.dtype)
    return out.index_add_(0, seg, src)
def _pad(x, pw):
    flat = tuple(v for pair in reversed(pw) for v in pair)
    return torch.nn.functional.pad(x, flat)

kernel_name = 'concat_heads__ssm_conv_s4d__mha_block__prompt'

def setup_inputs():

    _w = (lambda a: (lambda t: t.to(torch.bfloat16) if t.is_floating_point() else t)(torch.as_tensor(a)))
    def _A_setup():
        Bsz, L, D, N = 1, 2048, 640, 128
        return {
            "u": _w((lambda *s: torch.randn(*s, dtype=torch.bfloat16) * 0.05)(Bsz, L, D)),
            "log_A_real": _w((lambda *s: torch.randn(*s, dtype=torch.bfloat16) * 3.2)(D, N) * 0.5),
            "A_imag": _w((lambda *s: torch.randn(*s, dtype=torch.bfloat16) * 0.1)(D, N) * 3.14),
            "C_real": _w((lambda *s: torch.randn(*s, dtype=torch.bfloat16) * 1.6)(D, N) / N ** 0.5),
            "C_imag": _w((lambda *s: torch.randn(*s, dtype=torch.bfloat16) * 1.6)(D, N) / N ** 0.5),
            "log_dt": _w((lambda *s: torch.randn(*s, dtype=torch.bfloat16) * 0.1)(D) * 0.5 - 4.0),
            "D_skip": _w((lambda *s: torch.randn(*s, dtype=torch.bfloat16) * 0.1)(D)),
        }
    _A_in = _A_setup()

    def _B_setup():
        B, S, D, H = 1, 2048, 640, 32
        return {
            "x": _w((lambda *s: torch.randn(*s, dtype=torch.bfloat16) * 0.2)(B, S, D)),
            "wqkv": _w((lambda *s: torch.randn(*s, dtype=torch.bfloat16) * 0.2)(D, 3 * D)),
            "wo": _w((lambda *s: torch.randn(*s, dtype=torch.bfloat16) * 0.05)(D, D)),
        }
    _B_in = _B_setup()

    return {
        "A_u": _A_in["u"],
        "A_log_A_real": _A_in["log_A_real"],
        "A_A_imag": _A_in["A_imag"],
        "A_C_real": _A_in["C_real"],
        "A_C_imag": _A_in["C_imag"],
        "A_log_dt": _A_in["log_dt"],
        "A_D_skip": _A_in["D_skip"],
        "B_x": _B_in["x"],
        "B_wqkv": _B_in["wqkv"],
        "B_wo": _B_in["wo"],
    }

def reference(A_u, A_log_A_real, A_A_imag, A_C_real, A_C_imag, A_log_dt, A_D_skip, B_x, B_wqkv, B_wo):
    def _A_ref(u, log_A_real, A_imag, C_real, C_imag, log_dt, D_skip):
        L = u.shape[1]
        dt = torch.exp(log_dt)
        A = -torch.exp(log_A_real) + 1j * A_imag
        C = (C_real + 1j * C_imag) * dt[:, None]
        pos = (lambda x, dt: x.to(dt))(torch.arange(L), log_dt.dtype)
        P = torch.exp((A * dt[:, None])[:, :, None] * pos[None, None, :])
        K = 2.0 * torch.einsum("dn,dnl->dl", C, P).real
        L2 = 2 * L
        u_t = (lambda x, *d: x.permute(*d))(u, 0, 2, 1)
        Kf = (lambda x, *a, **k: torch.fft.rfft(x.float(), *a, **k))(K, L2)
        Uf = (lambda x, *a, **k: torch.fft.rfft(x.float(), *a, **k))(u_t, L2)
        y = (lambda x, *a, **k: torch.fft.irfft(x, *a, **k).to(torch.bfloat16))(Uf * Kf[None, :, :], L2)[:, :, :L]
        y = (lambda x, *d: x.permute(*d))(y, 0, 2, 1)
        return y + u * D_skip[None, None, :]

    def _B_ref(x, wqkv, wo):
        B, S, D = x.shape
        H = 32
        Dh = D // H
        qkv = (lambda x, *d: x.permute(*d))((x @ wqkv).reshape(B, S, 3, H, Dh), 2, 0, 3, 1, 4)
        q, k, v = qkv[0], qkv[1], qkv[2]
        scores = torch.einsum("bhqd,bhkd->bhqk", q, k) * (Dh ** -0.5)
        p = _softmax(scores, axis=-1)
        o = torch.einsum("bhqk,bhkd->bhqd", p, v)
        return (lambda x, *d: x.permute(*d))(o, 0, 2, 1, 3).reshape(B, S, D) @ wo

    a = _A_ref(A_u, A_log_A_real, A_A_imag, A_C_real, A_C_imag, A_log_dt, A_D_skip)
    b = _B_ref(B_x, B_wqkv, B_wo)
    return torch.concatenate([a, b], axis=-1)
```

```cpp
#include <hip/hip_runtime.h>
#include <hip/hip_bf16.h>
#include <stdint.h>

typedef float f32x16 __attribute__((ext_vector_type(16)));
typedef float f32x4 __attribute__((ext_vector_type(4)));
typedef float f32x2 __attribute__((ext_vector_type(2)));
typedef unsigned u32x4 __attribute__((ext_vector_type(4)));
typedef unsigned u32x2 __attribute__((ext_vector_type(2)));
typedef __bf16 bf16x8 __attribute__((ext_vector_type(8)));
typedef __bf16 bf16x4 __attribute__((ext_vector_type(4)));
typedef __bf16 bf16x2 __attribute__((ext_vector_type(2)));
typedef __bf16 bf16x16 __attribute__((ext_vector_type(16)));
typedef short s16x4 __attribute__((ext_vector_type(4)));
typedef unsigned short u16x2 __attribute__((ext_vector_type(2)));
typedef _Float16 f16x8 __attribute__((ext_vector_type(8)));
typedef _Float16 f16x2 __attribute__((ext_vector_type(2)));
#define LAS __attribute__((address_space(3)))
#define GAS __attribute__((address_space(1)))
#define DI __device__ __forceinline__

constexpr unsigned QT = 1280;
constexpr unsigned KVT = 2816;
constexpr size_t WS_Q = 0;
constexpr size_t WS_KV = WS_Q + 32u * 64u * QT;
constexpr size_t WS_O = WS_KV + 32u * 64u * KVT;
constexpr size_t WS_UT = WS_O + 32u * 64u * QT;
constexpr size_t WS_YT = WS_UT + 640u * 4096u;
constexpr int LDS_BYTES = 160 * 1024;

struct P {
  const char *u, *lar, *aim, *cre, *cim, *ldt, *dsk, *x, *wqkv, *wo;
  char *out, *ws;
};

DI float bf2f(unsigned short v) { return __uint_as_float((unsigned)v << 16); }
DI unsigned pk_bf16(float a, float b) { bf16x2 r; r[0] = (__bf16)a; r[1] = (__bf16)b; return __builtin_bit_cast(unsigned, r); }
DI unsigned pk_f16(float a, float b) { f16x2 r; r[0] = (_Float16)a; r[1] = (_Float16)b; return __builtin_bit_cast(unsigned, r); }
DI float rbf(float a) { return (float)(__bf16)a; }
template <class T> DI T ldsr(unsigned a) { return *(const LAS T*)(size_t)a; }
template <class T> DI void ldsw(unsigned a, T v) { *(LAS T*)(size_t)a = v; }
DI u32x2 ldtr(unsigned a) { return __builtin_bit_cast(u32x2, __builtin_amdgcn_ds_read_tr16_b64_v4i16((LAS s16x4*)(size_t)a)); }
DI u32x4 cat(u32x2 a, u32x2 b) { u32x4 r; r[0] = a[0]; r[1] = a[1]; r[2] = b[0]; r[3] = b[1]; return r; }
DI void bar() { asm volatile("s_waitcnt lgkmcnt(0)\n\ts_barrier" ::: "memory"); }
DI void dma16(const char* base, unsigned voff, unsigned lds_dst) {
  unsigned keep;
  asm volatile("s_mov_b32 %0, m0\n\ts_mov_b32 m0, %3\n\ts_nop 0\n\tglobal_load_lds_dwordx4 %1, %2\n\ts_mov_b32 m0, %0"
               : "=&s"(keep) : "v"(voff), "s"(base), "s"(lds_dst));
}
#define WAITV(n) asm volatile("s_waitcnt vmcnt(" #n ")" ::: "memory")

constexpr int G_NST = 4;
DI unsigned w_pos(unsigned q, unsigned chunk) {
  unsigned F = chunk / 5, r = chunk % 5, G, br;
  if (r == 2) { G = F; br = F; } else if (r == 4) { G = F; br = 3; }
  else { unsigned f = (r == 3) ? 2 : r; G = (f < F) ? f : f + 1; br = F; }
  return 16 * br + 4 * G + q;
}
DI unsigned vrem_pos(unsigned u) { return (u & 1) ? (u >> 1) : 2 + (u >> 1); }
DI void gemm_phase(const P& p, int rb, int cg, int wave, int lane) {
  const char* sbase; unsigned voff, pstep, sstep;
  if (wave < 2) {
    unsigned r = lane >> 3;
    voff = 0; sbase = p.x + (size_t)(rb * 128 + wave * 64) * 1280; pstep = 8 * 1280; sstep = 128;
    voff = r * 1280;
  } else {
    unsigned q = lane & 3, G = (lane >> 2) & 3, br = lane >> 4, chunk;
    if (br == 3) chunk = (G == 3) ? 0 : 5 * G + 4;
    else if (br == G) chunk = 5 * G + 2;
    else { unsigned f = (G < br) ? G : G - 1; chunk = 5 * br + (f == 2 ? 3 : f); }
    voff = q * 3840 + chunk * 16; sbase = p.wqkv + (size_t)(wave - 2) * 32 * 3840 + cg * 240; pstep = 4 * 3840; sstep = 64 * 3840;
  }
  unsigned voffA = voff, voffB = voff;
  if (wave < 2) { unsigned r = lane >> 3, c = lane & 7; voffA = voff + ((c ^ (r >> 1)) << 4); voffB = voff + ((c ^ (4 + (r >> 1))) << 4); }
  const unsigned ldst0 = wave * 8192;
  auto issue = [&](int s, int i) { dma16(sbase + (size_t)s * sstep + (size_t)i * pstep, (i & 1) ? voffB : voffA, (s % G_NST) * 32768 + ldst0 + i * 1024); };
#pragma unroll 1
  for (int s = 0; s < G_NST; s++) {
#pragma unroll
    for (int i = 0; i < 8; i++) issue(s, i);
  }
  const int wm = wave >> 1, wn = wave & 1, h = lane >> 5, tok = lane & 31;
  const unsigned xa = (wm * 64 + tok) * 128 + ((h ^ ((tok >> 1) & 7)) << 4);
  unsigned wa[2];
#pragma unroll
  for (int f = 0; f < 2; f++) {
    unsigned F = 2 * wn + f, n = 4 * ((lane >> 4) & 1) + (lane & 3), q = (lane >> 2) & 3;
    unsigned off = (F < 3) ? 2 * (20 * (2 * F + (n >> 2)) + 8 * (n & 1) + 4 * ((n >> 1) & 1)) : 2 * (20 * (n < 5 ? n : 5) + 16);
    wa[f] = 16384 + h * 2048 + w_pos(q, off >> 4) * 16 + (off & 8);
  }
  f32x16 acc[2][2];
#pragma unroll
  for (int a = 0; a < 2; a++)
#pragma unroll
    for (int b = 0; b < 2; b++)
#pragma unroll
      for (int i = 0; i < 16; i++) acc[a][b][i] = 0.f;
#pragma unroll 1
  for (int s = 0; s < 10; s++) {
    if (s == 0) WAITV(24); else if (s == 8) WAITV(8); else if (s == 9) WAITV(0); else WAITV(16);
    bar();
    const unsigned sb = (s % G_NST) * 32768;
    const bool more = (s >= 1 && s <= 10 - G_NST);
#pragma unroll
    for (int ks = 0; ks < 4; ks++) {
      u32x4 xf[2], wf[2];
#pragma unroll
      for (int t = 0; t < 2; t++) xf[t] = ldsr<u32x4>(sb + ((xa ^ (ks * 32)) + t * 4096));
#pragma unroll
      for (int f = 0; f < 2; f++) wf[f] = cat(ldtr(sb + wa[f] + ks * 4096), ldtr(sb + wa[f] + ks * 4096 + 1024));
      if (more) { issue(s - 1 + G_NST, 2 * ks); issue(s - 1 + G_NST, 2 * ks + 1); }
#pragma unroll
      for (int t = 0; t < 2; t++)
#pragma unroll
        for (int f = 0; f < 2; f++)
          acc[t][f] = __builtin_amdgcn_mfma_f32_32x32x16_bf16(__builtin_bit_cast(bf16x8, wf[f]), __builtin_bit_cast(bf16x8, xf[t]), acc[t][f], 0, 0, 0);
    }
  }
  const float qs = 0.32259784f;
#pragma unroll
  for (int t = 0; t < 2; t++) {
    const unsigned tile = rb * 4 + wm * 2 + t;
#pragma unroll
    for (int f = 0; f < 2; f++) {
      const int F = 2 * wn + f;
      if (f == 0 || wn == 0) {
#pragma unroll
        for (int e = 0; e < 2; e++) {
          const unsigned slot = cg * 6 + 2 * F + e, ty = slot >> 5, hd = slot & 31;
          const float sc = ty == 0 ? qs : 1.f;
          u32x4 v;
#pragma unroll
          for (int i = 0; i < 4; i++) v[i] = pk_bf16(acc[t][f][8 * e + 2 * i] * sc, acc[t][f][8 * e + 2 * i + 1] * sc);
          size_t o = ty == 0 ? WS_Q + (size_t)(hd * 64 + tile) * QT : WS_KV + (size_t)(hd * 64 + tile) * KVT + (ty == 2 ? 1280 : 0);
          unsigned lo = ty == 2 ? (tok >> 4) * 768 + (tok & 15) * 32 + h * 16 : tok * 16 + h * 512;
          *(GAS u32x4*)(p.ws + o + lo) = v;
        }
      } else {
#pragma unroll
        for (int Q = 0; Q < 3; Q++) {
          const unsigned slot = cg * 6 + 2 * Q + h, ty = slot >> 5, hd = slot & 31;
          const float sc = ty == 0 ? qs : 1.f;
          u32x4 v; v[0] = pk_bf16(acc[t][f][4 * Q] * sc, acc[t][f][4 * Q + 1] * sc); v[1] = pk_bf16(acc[t][f][4 * Q + 2] * sc, acc[t][f][4 * Q + 3] * sc);
          v[2] = 0x00003f80u; v[3] = 0;
          unsigned o = ty == 0 ? (unsigned)WS_Q + (hd * 64 + tile) * QT : (unsigned)WS_KV + (hd * 64 + tile) * KVT;
          if (ty == 2) *(GAS u32x4*)(p.ws + o + 1280 + (tok >> 4) * 768 + 512 + vrem_pos((tok >> 2) & 3) * 64 + (tok & 3) * 16) = v;
          else { u32x2 w; w[0] = v[0]; w[1] = v[1]; *(GAS u32x2*)(p.ws + o + 1024 + tok * 8) = w; }
        }
      }
    }
  }
}

DI void utrans_load(const P& p, int rj, int cj, int t256, unsigned lb) {
#pragma unroll
  for (int i = 0; i < 3; i++) {
    int c = t256 + 256 * i;
    if (c < 640) { int row = c / 10, ch = c % 10; ldsw<u32x4>(lb + row * 176 + ch * 16, *(const GAS u32x4*)(p.u + (size_t)(rj * 64 + row) * 1280 + cj * 160 + ch * 16)); }
  }
}
DI void utrans_store(const P& p, int rj, int cj, int w4, int lane, unsigned lb) {
#pragma unroll
  for (int i = 0; i < 3; i++) {
    int task = w4 + 4 * i;
    if (task < 10) {
      int cb = task >> 1, oct = (task & 1) * 4 + (lane >> 4), q = (lane >> 2) & 3, pp = lane & 3;
      unsigned a = lb + (oct * 8 + q) * 176 + cb * 32 + pp * 8;
      u32x4 v = cat(ldtr(a), ldtr(a + 4 * 176));
      *(GAS u32x4*)(p.ws + WS_UT + (size_t)(cj * 80 + cb * 16 + (lane & 15)) * 4096 + (rj * 64 + oct * 8) * 2) = v;
    }
  }
}

struct cplx { float r, i; };
DI cplx cmul(cplx a, cplx b) { cplx c; c.r = fmaf(a.r, b.r, -a.i * b.i); c.i = fmaf(a.r, b.i, a.i * b.r); return c; }
DI cplx cexp_d(float ar2, float air, float d) {
  float m = __builtin_amdgcn_exp2f(ar2 * d), th = __builtin_amdgcn_fractf(air * d);
  cplx c; c.r = m * __builtin_amdgcn_cosf(th); c.i = m * __builtin_amdgcn_sinf(th); return c;
}
constexpr unsigned KG_ROW = 272, KG_BYTES = 54 * KG_ROW;
DI int kgen(const P& p, int ch, int lane, unsigned scr, unsigned kc) {
  const float dt = rbf(expf(bf2f(((const GAS unsigned short*)p.ldt)[ch])));
  float ar2[2], air[2], cr[2], ci[2];
#pragma unroll
  for (int s = 0; s < 2; s++) {
    const int o = ch * 128 + s * 64 + lane;
    ar2[s] = -rbf(expf(bf2f(((const GAS unsigned short*)p.lar)[o]))) * dt * 1.44269504f;
    air[s] = bf2f(((const GAS unsigned short*)p.aim)[o]) * dt * 0.15915494f;
    cr[s] = bf2f(((const GAS unsigned short*)p.cre)[o]) * dt * 2.f;
    ci[s] = bf2f(((const GAS unsigned short*)p.cim)[o]) * dt * 2.f;
  }
  float mc = fmaxf(fmaxf(fabsf(cr[0]), fabsf(ci[0])), fmaxf(fabsf(cr[1]), fabsf(ci[1])));
#pragma unroll
  for (int o = 32; o > 0; o >>= 1) mc = fmaxf(mc, __shfl_xor(mc, o));
  const int e = (mc > 0.f && mc < 3e38f) ? 8 - __builtin_amdgcn_frexp_expf(mc) : 0;
  f32x16 acc;
#pragma unroll
  for (int i = 0; i < 16; i++) acc[i] = 0.f;
  const unsigned wr = scr + lane * 4;
  const int r = lane & 31, h = lane >> 5;
  const unsigned ga = scr + (r < 21 ? r : 21) * KG_ROW + h * 16, ea = scr + (22 + r) * KG_ROW + h * 16;
  ldsw<unsigned>(wr + 21 * KG_ROW, 0u);
#pragma unroll 1
  for (int s = 0; s < 2; s++) {
    const float a2 = s ? ar2[1] : ar2[0], ai = s ? air[1] : air[0];
    cplx c; c.r = ldexpf(s ? cr[1] : cr[0], e); c.i = ldexpf(s ? ci[1] : ci[0], e);
    const cplx w1 = cexp_d(a2, ai, 1.f), w8 = cexp_d(a2, ai, 8.f), w256 = cexp_d(a2, ai, 256.f);
    auto putG = [&](int row, cplx g) { ldsw<unsigned>(wr + row * KG_ROW, pk_f16(g.r, -g.i)); };
    cplx x; x.r = 1.f; x.i = 0.f;
    ldsw<unsigned>(wr + 22 * KG_ROW, 0x00003c00u);
    x = w8;
#pragma unroll
    for (int j = 1; j < 32; j++) { ldsw<unsigned>(wr + (22 + j) * KG_ROW, pk_f16(x.r, x.i)); if (j < 31) x = cmul(x, w8); }
    const cplx w2 = cmul(w1, w1), w4 = cmul(w2, w2);
    cplx g = c;
#pragma unroll
    for (int i = 0; i < 8; i++) { putG(i, g); if (i < 7) g = cmul(g, w1); }
    cplx b = cmul(c, w256); g = b;
#pragma unroll
    for (int i = 0; i < 4; i++) { putG(8 + i, g); if (i < 3) g = cmul(g, w2); }
    b = cmul(b, w256); putG(12, b); putG(13, cmul(b, w4));
    b = cmul(b, w256); putG(14, b); putG(15, cmul(b, w4));
#pragma unroll
    for (int i = 0; i < 5; i++) { b = cmul(b, w256); putG(16 + i, b); }
#pragma unroll
    for (int k = 0; k < 8; k++)
      acc = __builtin_amdgcn_mfma_f32_32x32x16_f16(__builtin_bit_cast(f16x8, ldsr<u32x4>(ga + k * 32)), __builtin_bit_cast(f16x8, ldsr<u32x4>(ea + k * 32)), acc, 0, 0, 0);
  }
#pragma unroll
  for (int i = 0; i < 12; i++) ldsw<_Float16>(kc + (((i & 3) + 8 * (i >> 2) + 4 * h) * 32 + r) * 2, (_Float16)acc[i]);
  return e;
}
DI unsigned kc_index(int x) {
  if (x < 0) return 21 * 64;
  const int pp = (int)rbf((float)x);
  int i, j = (pp & 255) >> 3;
  if (pp < 256) i = pp & 7; else if (pp < 512) i = 8 + ((pp & 7) >> 1); else if (pp < 1024) i = 8 + 2 * (pp >> 8) + ((pp & 7) >> 2); else i = 12 + (pp >> 8);
  return (i * 32 + j) * 2;
}
constexpr unsigned W4_BYTES = 2064 * 8, U_BYTES = (256 + 2048) * 2;
DI void build_windows(unsigned kc, unsigned w4, int tid, const unsigned (&ix)[7]) {
  unsigned k[7];
#pragma unroll
  for (int i = 0; i < 7; i++) k[i] = ldsr<unsigned short>(kc + ix[i]);
  auto D = [&](int y) { return k[y + 3] | (k[y + 2] << 16); };
  u32x4 a, b;
  a[0] = D(0); a[1] = D(-2); a[2] = D(1); a[3] = D(-1);
  b[0] = D(2); b[1] = D(0); b[2] = D(3); b[3] = D(1);
  ldsw<u32x4>(w4 + 128 + tid * 32, a); ldsw<u32x4>(w4 + 128 + tid * 32 + 16, b);
  if (tid < 8) { u32x4 z = {0, 0, 0, 0}; ldsw<u32x4>(w4 + tid * 16, z); }
}
DI int stage_u(const P& p, int ch, int lane, unsigned ub) {
  u32x4 v[4];
#pragma unroll
  for (int i = 0; i < 4; i++) v[i] = *(const GAS u32x4*)(p.ws + WS_UT + (size_t)ch * 4096 + i * 1024 + lane * 16);
  u16x2 m2 = {0, 0};
#pragma unroll
  for (int i = 0; i < 4; i++)
#pragma unroll
    for (int j = 0; j < 4; j++) m2 = __builtin_elementwise_max(m2, __builtin_bit_cast(u16x2, v[i][j] & 0x7fff7fffu));
  unsigned m = max((unsigned)m2[0], (unsigned)m2[1]);
#pragma unroll
  for (int o = 32; o > 0; o >>= 1) m = max(m, (unsigned)__shfl_xor((int)m, o));
  const int B = max((int)(m >> 7) - 30, 0);
  u16x2 b2; b2[0] = b2[1] = (unsigned short)(B << 7);
#pragma unroll
  for (int i = 0; i < 4; i++) {
    u32x4 w;
#pragma unroll
    for (int j = 0; j < 4; j++) {
      u16x2 t = __builtin_elementwise_sub_sat(__builtin_bit_cast(u16x2, v[i][j] & 0x7fff7fffu), b2);
      w[j] = (__builtin_bit_cast(unsigned, t) << 3) | (v[i][j] & 0x80008000u);
    }
    ldsw<u32x4>(ub + 512 + i * 1024 + lane * 16, w);
  }
  if (lane < 32) { u32x4 z = {0, 0, 0, 0}; ldsw<u32x4>(ub + lane * 16, z); }
  return B;
}
DI void conv_mma(unsigned w4b, unsigned ub, int w4, int lane, f32x4 (&acc)[8]) {
  const int b = lane & 15, g = lane >> 4;
#pragma unroll
  for (int t = 0; t < 8; t++) acc[t] = f32x4{0.f, 0.f, 0.f, 0.f};
#pragma unroll 1
  for (int rr = 0; rr < 2; rr++) {
    const int r = 16 + 32 * (2 * w4 + rr);
    const unsigned aa = w4b + (16 + r + b - 8 * g - 4) * 8, ba = ub + (256 - r + 16 * b + 8 * g) * 2;
    u32x4 B[8];
#pragma unroll
    for (int m = 0; m < 8; m++) B[m] = ldsr<u32x4>(ba + m * 512);
#pragma unroll
    for (int i = 0; i < 8; i++) {
      const u32x4 A = cat(ldsr<u32x2>(aa + i * 2048 + 32), ldsr<u32x2>(aa + i * 2048));
#pragma unroll
      for (int m = 0; m + i < 8; m++)
        acc[i + m] = __builtin_amdgcn_mfma_f32_16x16x32_f16(__builtin_bit_cast(f16x8, A), __builtin_bit_cast(f16x8, B[m]), acc[i + m], 0, 0, 0);
    }
  }
}

#define XS(x) #x
#define S(x) XS(x)
#define EXP4(b, i) "v_exp_f32 v[" S(b) "+" S(i) "], v[" S(b) "+" S(i) "]\n v_exp_f32 v[" S(b) "+" S(i) "+1], v[" S(b) "+" S(i) "+1]\n" \
                   "v_exp_f32 v[" S(b) "+" S(i) "+2], v[" S(b) "+" S(i) "+2]\n v_exp_f32 v[" S(b) "+" S(i) "+3], v[" S(b) "+" S(i) "+3]\n"
#define STEP(cur, nxt, T) \
  "s_waitcnt lgkmcnt(4)\n" \
  "v_smfmac_f32_32x32x32_bf16 %[O], v[68:71], v[" S(nxt) "+8:" S(nxt) "+15], %[idx]\n" \
  "ds_read_b64_tr_b16 v[68:69], %[aV] offset:2816*" S(T) "+768\n" \
  "ds_read_b64_tr_b16 v[70:71], %[aV2] offset:2816*" S(T) "+768\n" \
  "s_nop 0\n" \
  EXP4(cur, 0) \
  "s_waitcnt lgkmcnt(5)\n" \
  "v_mfma_f32_32x32x16_bf16 v[" S(nxt) ":" S(nxt) "+15], v[72:75], %[Qm], %[NM]\n" \
  EXP4(cur, 4) \
  "ds_read_b128 v[72:75], %[aK] offset:2816*(" S(T) "+2)\n" \
  "s_waitcnt lgkmcnt(3)\n" \
  "v_smfmac_f32_32x32x32_bf16 %[O], v[64:67], v[" S(cur) ":" S(cur) "+7], %[idx]\n" \
  EXP4(cur, 8) \
  "v_mfma_f32_32x32x8_bf16 v[" S(nxt) ":" S(nxt) "+15], v[76:77], %[Qr], v[" S(nxt) ":" S(nxt) "+15]\n" \
  EXP4(cur, 12) \
  "ds_read_b64 v[76:77], %[aKr] offset:2816*(" S(T) "+2)\n" \
  "ds_read_b64_tr_b16 v[64:65], %[aV] offset:2816*(" S(T) "+1)\n" \
  "ds_read_b64_tr_b16 v[66:67], %[aV2] offset:2816*(" S(T) "+1)\n"
#define STEP2(T0, T1) STEP(32, 48, T0) STEP(48, 32, T1)

DI void kv_issue(const char* src, int first_tile, int ntiles, unsigned lds_pos, int wave, int lane) {
  const int np = ntiles * (int)KVT / 1024 + ((ntiles * KVT) % 1024 ? 1 : 0);
#pragma unroll 1
  for (int pc = wave; pc < np; pc += 8) dma16(src + (size_t)first_tile * KVT + pc * 1024, lane * 16, lds_pos + pc * 1024);
}

DI void attn_phase(const P& p, int head, int chunk, int wave, int lane) {
  const char* kv = p.ws + WS_KV + (size_t)head * 64 * KVT;
  const int h = lane >> 5, q = lane & 31, qt = chunk * 8 + wave;
  const char* qp = p.ws + WS_Q + (size_t)(head * 64 + qt) * QT;
  u32x4 Qm = *(const GAS u32x4*)(qp + lane * 16);
  u32x2 Qr = *(const GAS u32x2*)(qp + 1024 + q * 8);
  if (h) { Qr[0] = 0; Qr[1] = 0; }
  asm volatile("" : "+v"(Qm), "+v"(Qr));
  kv_issue(kv, 0, 20, 0, wave, lane);
  WAITV(0); bar();
  kv_issue(kv, 20, 30, 20 * KVT, wave, lane);
  unsigned aK = lane * 16, aKr = 1024 + q * 8;
  unsigned aV, aV2;
  { const int blk = (lane >> 4) & 1, r = (lane >> 2) & 3, c = lane & 3;
    aV = 1280 + (blk ? 512 + vrem_pos(2 * h) * 64 + r * 16 + (c & 1) * 8 : (8 * h + r) * 32 + c * 8);
    aV2 = 1280 + (blk ? 512 + vrem_pos(2 * h + 1) * 64 + r * 16 + (c & 1) * 8 : (8 * h + 4 + r) * 32 + c * 8); }
  f32x16 NM, O, S0, S1;
  u32x4 V0, V1, Km; u32x2 Kr;
  {
    Km = ldsr<u32x4>(aK); Kr = ldsr<u32x2>(aKr);
    f32x16 z;
#pragma unroll
    for (int i = 0; i < 16; i++) z[i] = 0.f;
    f32x16 s = __builtin_amdgcn_mfma_f32_32x32x16_bf16(__builtin_bit_cast(bf16x8, Km), __builtin_bit_cast(bf16x8, Qm), z, 0, 0, 0);
    s = __builtin_amdgcn_mfma_f32_32x32x8bf16_1k(__builtin_bit_cast(s16x4, Kr), __builtin_bit_cast(s16x4, Qr), s, 0, 0, 0);
    float m = s[0];
#pragma unroll
    for (int i = 1; i < 16; i++) m = fmaxf(m, s[i]);
    m = fmaxf(m, __shfl_xor(m, 32));
#pragma unroll
    for (int i = 0; i < 16; i++) { NM[i] = -m; S0[i] = s[i] - m; S1[i] = 0.f; O[i] = 0.f; }
    Km = ldsr<u32x4>(aK + KVT); Kr = ldsr<u32x2>(aKr + KVT);
    V0 = cat(ldtr(aV), ldtr(aV2));
    V1 = u32x4{0, 0, 0, 0};
  }
  unsigned idx = 0xddddu;
#pragma unroll 1
  for (int it = 0; it < 4; it++) {
    if (it == 1) { WAITV(0); bar(); kv_issue(kv, 48, 16, 0, wave, lane); }
    if (it == 3) { WAITV(0); bar(); }
    asm volatile("s_nop 1\n" STEP2(0, 1) STEP2(2, 3) STEP2(4, 5) STEP2(6, 7) STEP2(8, 9) STEP2(10, 11) STEP2(12, 13) STEP2(14, 15)
                 "s_waitcnt lgkmcnt(0)\n"
                 : [O] "+v"(O), "+{v[32:47]}"(S0), "+{v[48:63]}"(S1), "+{v[64:67]}"(V0), "+{v[68:71]}"(V1), "+{v[72:75]}"(Km), "+{v[76:77]}"(Kr)
                 : [NM] "v"(NM), [Qm] "v"(Qm), [Qr] "v"(Qr), [idx] "v"(idx), [aK] "v"(aK), [aKr] "v"(aKr), [aV] "v"(aV), [aV2] "v"(aV2)
                 : "memory");
    const unsigned d = it == 2 ? (unsigned)(-32 * (int)KVT) : 16 * KVT;
    aK += d; aKr += d; aV += d; aV2 += d;
  }
  asm volatile("s_nop 1\n v_smfmac_f32_32x32x32_bf16 %[O], v[68:71], v[56:63], %[idx]\n s_nop 15\n"
               : [O] "+v"(O) : "{v[48:63]}"(S1), "{v[68:71]}"(V1), [idx] "v"(idx));
  const float l = __shfl(O[8], lane | 32);
  const float inv = 1.f / l;
  char* op = p.ws + WS_O + (size_t)(head * 64 + qt) * QT;
  u32x4 v;
#pragma unroll
  for (int i = 0; i < 4; i++) v[i] = pk_bf16(O[2 * i] * inv, O[2 * i + 1] * inv);
  *(GAS u32x4*)(op + lane * 16) = v;
  if (!h) { u32x2 w; w[0] = pk_bf16(O[8] * inv, O[9] * inv); w[1] = pk_bf16(O[10] * inv, O[11] * inv); *(GAS u32x2*)(op + 1024 + q * 8) = w; }
}

DI void ytrans_load(const P& p, int rj, int cj, int tid, unsigned lb) {
#pragma unroll
  for (int i = 0; i < 2; i++) {
    int c = tid + 512 * i;
    if (c < 640) { int ch = c >> 3, o = c & 7; ldsw<u32x4>(lb + ch * 144 + o * 16, *(const GAS u32x4*)(p.ws + WS_YT + (size_t)(cj * 80 + ch) * 4096 + rj * 128 + o * 16)); }
  }
}
DI void ytrans_store(const P& p, int rj, int cj, int wave, int lane, unsigned lb) {
#pragma unroll
  for (int i = 0; i < 2; i++) {
    int task = wave + 8 * i;
    if (task < 10) {
      int gt = task * 4 + (lane >> 4), rbk = gt / 10, oct = gt % 10, q = (lane >> 2) & 3, pp = lane & 3;
      unsigned a = lb + (oct * 8 + q) * 144 + rbk * 32 + pp * 8;
      u32x4 y = cat(ldtr(a), ldtr(a + 4 * 144));
      int row = rj * 64 + rbk * 16 + (lane & 15);
      u32x4 uu = *(const GAS u32x4*)(p.u + (size_t)row * 1280 + cj * 160 + oct * 16);
      u32x4 dd = *(const GAS u32x4*)(p.dsk + cj * 160 + oct * 16);
      u32x4 o;
#pragma unroll
      for (int j = 0; j < 4; j++) {
        float lo = __uint_as_float(y[j] << 16) + rbf(__uint_as_float(uu[j] << 16) * __uint_as_float(dd[j] << 16));
        float hi = __uint_as_float(y[j] & 0xffff0000u) + rbf(__uint_as_float(uu[j] & 0xffff0000u) * __uint_as_float(dd[j] & 0xffff0000u));
        o[j] = pk_bf16(lo, hi);
      }
      *(GAS u32x4*)(p.out + (size_t)row * 2560 + cj * 160 + oct * 16) = o;
    }
  }
}
DI void wo_issue(const P& p, int cj, int wave, int lane, unsigned lb) {
#pragma unroll 1
  for (int pc = wave; pc < 100; pc += 8) { int c = pc * 64 + lane, row = c / 10, ch = c % 10; dma16(p.wo + cj * 160, row * 1280 + ch * 16, lb + pc * 1024); }
}
DI void oproj(const P& p, int rj, int cj, int wave, int lane, unsigned wb, unsigned red) {
  const int wm = wave & 3, wk = wave >> 2, g = lane >> 4, tk = lane & 15;
  const int tile = rj * 2 + (wm >> 1), tok = (wm & 1) * 16 + tk;
  const char* ob = p.ws + WS_O + (size_t)tile * QT;
  u32x4 B[10];
  const int q = (lane >> 2) & 3, pp = lane & 3;
  const unsigned mo = ((g >> 1) * 64 * QT + (g & 1) * 512 + tok * 16), ro = g * 64 * QT + 1024 + tok * 8;
  if (wk == 0) {
#pragma unroll
    for (int s = 0; s < 10; s++) B[s] = *(const GAS u32x4*)(ob + (size_t)s * 128 * QT + mo);
  } else {
#pragma unroll
    for (int s = 0; s < 6; s++) B[s] = *(const GAS u32x4*)(ob + (size_t)(10 + s) * 128 * QT + mo);
#pragma unroll
    for (int s = 0; s < 4; s++) B[6 + s] = cat(*(const GAS u32x2*)(ob + (size_t)s * 512 * QT + ro), *(const GAS u32x2*)(ob + (size_t)s * 512 * QT + 256 * QT + ro));
  }
  f32x4 acc[5];
#pragma unroll
  for (int c = 0; c < 5; c++) acc[c] = f32x4{0.f, 0.f, 0.f, 0.f};
  const unsigned am = wb + ((g >> 1) * 20 + (g & 1) * 4 + q) * 160 + pp * 8 + wk * 400 * 160, ar = wb + (g * 20 + 16 + q) * 160 + pp * 8;
#pragma unroll
  for (int s = 0; s < 10; s++) {
    const bool rem = (wk == 1 && s >= 6);
    const unsigned a0 = rem ? ar + (s - 6) * 160 * 160 : am + s * 40 * 160, a1 = a0 + (rem ? 80 * 160 : 8 * 160);
#pragma unroll
    for (int c = 0; c < 5; c++)
      acc[c] = __builtin_amdgcn_mfma_f32_16x16x32_bf16(__builtin_bit_cast(bf16x8, cat(ldtr(a0 + c * 32), ldtr(a1 + c * 32))), __builtin_bit_cast(bf16x8, B[s]), acc[c], 0, 0, 0);
  }
  const unsigned ra = red + wm * 5120 + lane * 16;
#pragma unroll
  for (int c = 0; c < 5; c++) if ((c < 3) == (wk == 1)) ldsw<f32x4>(ra + c * 1024, acc[c]);
  bar();
  char* o = p.out + (size_t)(rj * 64 + wm * 16 + tk) * 2560 + 1280 + cj * 160 + g * 8;
#pragma unroll
  for (int c = 0; c < 5; c++)
    if ((c < 3) == (wk == 0)) {
      f32x4 t = acc[c] + ldsr<f32x4>(ra + c * 1024);
      u32x2 w; w[0] = pk_bf16(t[0], t[1]); w[1] = pk_bf16(t[2], t[3]);
      *(GAS u32x2*)(o + c * 32) = w;
    }
}

__global__ void __launch_bounds__(512, 2) k1(P p) {
  extern __shared__ __attribute__((aligned(16))) char lds[];
  const int b = blockIdx.x, xcd = b & 7, i = b >> 3, tid = threadIdx.x;
  const int wave = __builtin_amdgcn_readfirstlane(tid >> 6), lane = tid & 63;
  if (wave < 4) gemm_phase(p, (xcd >> 1) * 4 + (i >> 3), (xcd & 1) * 8 + (i & 7), wave, lane);
  else {
    const int rj = xcd * 4 + (i >> 3), cj = i & 7;
    utrans_load(p, rj, cj, tid - 256, 131072);
    bar();
    utrans_store(p, rj, cj, wave - 4, lane, 131072);
#pragma unroll 1
    for (int s = 1; s < 10; s++) bar();
  }
}

__global__ void __launch_bounds__(512, 2) k2(P p) {
  extern __shared__ __attribute__((aligned(16))) char lds[];
  const int b = blockIdx.x, xcd = b & 7, i = b >> 3, tid = threadIdx.x;
  const int wave = __builtin_amdgcn_readfirstlane(tid >> 6), lane = tid & 63;
  const int nch = b < 128 ? 3 : 2, ch0 = b < 128 ? 3 * b : 384 + 2 * (b - 128);
  constexpr unsigned L_W4 = 0, L_U = 3 * W4_BYTES, L_KC = L_U + 3 * U_BYTES, L_E = L_KC + 3 * 1536, L_S = L_E + 64;
  if (wave < nch) { int e = kgen(p, ch0 + wave, lane, L_S + wave * KG_BYTES, L_KC + wave * 1536); if (lane == 0) ldsw<int>(L_E + wave * 4, e); }
  else if (wave >= 4 && wave - 4 < nch) { int B = stage_u(p, ch0 + wave - 4, lane, L_U + (wave - 4) * U_BYTES); if (lane == 0) ldsw<int>(L_E + 16 + (wave - 4) * 4, B); }
  unsigned ix[7];
#pragma unroll
  for (int j = 0; j < 7; j++) ix[j] = kc_index(4 * tid - 3 + j);
  bar();
#pragma unroll 1
  for (int c = 0; c < nch; c++) build_windows(L_KC + c * 1536, L_W4 + c * W4_BYTES, tid, ix);
  bar();
#pragma unroll 1
  for (int rd = 0; rd < 2; rd++) {
    const int c = rd * 2 + (wave >> 2), w4 = wave & 3;
    const bool act = c < nch;
    f32x4 acc[8];
    const unsigned red = L_S + (wave >> 2) * 32768;
    if (act) {
      conv_mma(L_W4 + c * W4_BYTES, L_U + c * U_BYTES, w4, lane, acc);
#pragma unroll
      for (int t = 0; t < 8; t++) ldsw<f32x4>(red + (w4 * 8 + t) * 1024 + lane * 16, acc[t]);
    }
    bar();
    if (act) {
      const int sh = ldsr<int>(L_E + 16 + c * 4) - 112 - ldsr<int>(L_E + c * 4);
#pragma unroll
      for (int tt = 0; tt < 2; tt++) {
        const int t = 2 * w4 + tt;
        f32x4 y = ldsr<f32x4>(red + t * 1024 + lane * 16);
#pragma unroll
        for (int w = 1; w < 4; w++) y += ldsr<f32x4>(red + (w * 8 + t) * 1024 + lane * 16);
        u32x2 o; o[0] = pk_bf16(ldexpf(y[0], sh), ldexpf(y[1], sh)); o[1] = pk_bf16(ldexpf(y[2], sh), ldexpf(y[3], sh));
        *(GAS u32x2*)(p.ws + WS_YT + (size_t)(ch0 + c) * 4096 + (256 * t + 16 * (lane & 15) + 4 * (lane >> 4)) * 2) = o;
      }
    }
    bar();
  }
  attn_phase(p, xcd * 4 + (i >> 3), i & 7, wave, lane);
}

__global__ void __launch_bounds__(512, 2) k3(P p) {
  extern __shared__ __attribute__((aligned(16))) char lds[];
  const int b = blockIdx.x, xcd = b & 7, i = b >> 3, tid = threadIdx.x;
  const int wave = __builtin_amdgcn_readfirstlane(tid >> 6), lane = tid & 63;
  const int rj = xcd * 4 + (i >> 3), cj = i & 7;
  ytrans_load(p, rj, cj, tid, 102400);
  wo_issue(p, cj, wave, lane, 0);
  bar();
  ytrans_store(p, rj, cj, wave, lane, 102400);
  WAITV(0); bar();
  oproj(p, rj, cj, wave, lane, 0, 114688);
}

extern "C" void kernel_launch(void* const* d_in, const int* in_sizes, int n_in, void* d_out, int out_size, void* d_ws, size_t ws_size,
                              hipStream_t stream) {
  P p;
  p.u = (const char*)d_in[0]; p.lar = (const char*)d_in[1]; p.aim = (const char*)d_in[2]; p.cre = (const char*)d_in[3];
  p.cim = (const char*)d_in[4]; p.ldt = (const char*)d_in[5]; p.dsk = (const char*)d_in[6]; p.x = (const char*)d_in[7];
  p.wqkv = (const char*)d_in[8]; p.wo = (const char*)d_in[9]; p.out = (char*)d_out; p.ws = (char*)d_ws;
  (void)hipFuncSetAttribute((const void*)k1, hipFuncAttributeMaxDynamicSharedMemorySize, LDS_BYTES);
  (void)hipFuncSetAttribute((const void*)k2, hipFuncAttributeMaxDynamicSharedMemorySize, LDS_BYTES);
  (void)hipFuncSetAttribute((const void*)k3, hipFuncAttributeMaxDynamicSharedMemorySize, LDS_BYTES);
  k1<<<256, 512, LDS_BYTES, stream>>>(p);
  k2<<<256, 512, LDS_BYTES, stream>>>(p);
  k3<<<256, 512, LDS_BYTES, stream>>>(p);
}
```

```cpp
#include <hip/hip_runtime.h>
#include <hip/hip_bf16.h>
#include <stdint.h>

typedef float f32x16 __attribute__((ext_vector_type(16)));
typedef float f32x8 __attribute__((ext_vector_type(8)));
typedef float f32x4 __attribute__((ext_vector_type(4)));
typedef float f32x2 __attribute__((ext_vector_type(2)));
typedef unsigned u32x4 __attribute__((ext_vector_type(4)));
typedef unsigned u32x2 __attribute__((ext_vector_type(2)));
typedef __bf16 bf16x8 __attribute__((ext_vector_type(8)));
typedef __bf16 bf16x2 __attribute__((ext_vector_type(2)));
typedef __bf16 bf16x16 __attribute__((ext_vector_type(16)));
typedef short s16x4 __attribute__((ext_vector_type(4)));
typedef unsigned short u16x2 __attribute__((ext_vector_type(2)));
typedef _Float16 f16x8 __attribute__((ext_vector_type(8)));
typedef _Float16 f16x2 __attribute__((ext_vector_type(2)));
#define LAS __attribute__((address_space(3)))
#define GAS __attribute__((address_space(1)))
#define DI __device__ __forceinline__
typedef __amdgpu_buffer_rsrc_t rsrc_t;

constexpr unsigned QT = 1280;
constexpr unsigned PAIR = 5632;
constexpr unsigned POS = 2 * PAIR;
constexpr unsigned WS_Q = 0;
constexpr unsigned WS_KV = WS_Q + 32u * 64u * QT;
constexpr unsigned WS_O = WS_KV + 32u * 16u * POS;
constexpr unsigned WS_UT = WS_O + 32u * 64u * QT;
constexpr unsigned WS_YT = WS_UT + 640u * 4096u;
constexpr unsigned WS_F = WS_YT + 640u * 4096u;
constexpr unsigned F_U = WS_F, F_Q = WS_F + 2048, F_Y = WS_F + 4096, F_O = WS_F + 6144, F_E = WS_F + 8192;
constexpr unsigned WS_END = WS_F + 16384;
constexpr int LDS_BYTES = 160 * 1024;

constexpr unsigned W4_BYTES = 2064 * 8, U_BYTES = (256 + 2048) * 2;
constexpr unsigned L_MISC = 98304;
constexpr unsigned L_W4 = LDS_BYTES - 3 * W4_BYTES - 3 * U_BYTES, L_U = L_W4 + 3 * W4_BYTES;
constexpr unsigned L_PAR = L_U + 10240;
constexpr unsigned L_KC = 65536;
constexpr unsigned L_RED = 5 * POS + 1024;
constexpr unsigned L_WO = 4 * POS;
static_assert(L_MISC + 64 <= L_W4 && L_RED + 16384 + 24576 <= L_MISC && L_WO + 102400 <= LDS_BYTES, "lds map");

struct P {
  const char *u, *lar, *aim, *cre, *cim, *ldt, *dsk, *x, *wqkv, *wo;
  char *out, *ws;
};

DI float bf2f(unsigned short v) { return __uint_as_float((unsigned)v << 16); }
DI unsigned pk_bf16(float a, float b) { bf16x2 r; r[0] = (__bf16)a; r[1] = (__bf16)b; return __builtin_bit_cast(unsigned, r); }
DI unsigned pk_f16(float a, float b) { f16x2 r; r[0] = (_Float16)a; r[1] = (_Float16)b; return __builtin_bit_cast(unsigned, r); }
DI float rbf(float a) { return (float)(__bf16)a; }
template <class T> DI T ldsr(unsigned a) { return *(const LAS T*)(size_t)a; }
template <class T> DI void ldsw(unsigned a, T v) { *(LAS T*)(size_t)a = v; }
DI u32x2 ldtr(unsigned a) { return __builtin_bit_cast(u32x2, __builtin_amdgcn_ds_read_tr16_b64_v4i16((LAS s16x4*)(size_t)a)); }
DI u32x4 cat(u32x2 a, u32x2 b) { u32x4 r; r[0] = a[0]; r[1] = a[1]; r[2] = b[0]; r[3] = b[1]; return r; }
DI u32x2 call_tag() { const u32x2 e = ldsr<u32x2>(L_MISC); return u32x2{e[0] * 0x9E3779B1u + 0x7F4A7C15u, e[1] * 0x85EBCA77u + 0x165667B1u}; }
DI void bar() { asm volatile("s_waitcnt lgkmcnt(0)\n\ts_barrier" ::: "memory"); }
#define WAITV(n) asm volatile("s_waitcnt vmcnt(" #n ")" ::: "memory")
constexpr int SC1 = 16;
DI u32x4 ld16(rsrc_t r, unsigned o) { return __builtin_amdgcn_raw_buffer_load_b128(r, o, 0, SC1); }
DI u32x2 ld8(rsrc_t r, unsigned o) { return __builtin_amdgcn_raw_buffer_load_b64(r, o, 0, SC1); }
DI void st16(rsrc_t r, unsigned o, u32x4 v) { __builtin_amdgcn_raw_buffer_store_b128(v, r, o, 0, SC1); }
DI void st8(rsrc_t r, unsigned o, u32x2 v) { __builtin_amdgcn_raw_buffer_store_b64(v, r, o, 0, SC1); }
DI bool tags_ready(rsrc_t r, unsigned o, u32x2 tag) {
  u32x2 v = ld8(r, o);
  asm volatile("" : "+v"(v) :: "memory");
  return __builtin_amdgcn_ballot_w64(v[0] != tag[0] || v[1] != tag[1]) == 0;
}
DI void tags_wait(rsrc_t r, unsigned o, u32x2 tag) {
#pragma unroll 1
  for (int i = 0; i < (1 << 16); i++) { if (tags_ready(r, o, tag)) break; __builtin_amdgcn_s_sleep(1); }
}
DI void set_m0(unsigned v) { asm volatile("s_mov_b32 m0, %0\n\ts_nop 0" :: "s"(v)); }
template <int OFF, bool DEV> DI void dma(const char* base, unsigned voff) {
  if (DEV) asm volatile("global_load_lds_dwordx4 %0, %1 offset:%2 sc1" :: "v"(voff), "s"(base), "i"(OFF));
  else asm volatile("global_load_lds_dwordx4 %0, %1 offset:%2" :: "v"(voff), "s"(base), "i"(OFF));
}
#define STAMP(i)
#define STAMPX(i)
#define STAMPY(i)
#define XS(x) #x
#define S(x) XS(x)

DI unsigned vtail_pos(unsigned u) { return (u & 1) ? (u >> 1) : 2 + (u >> 1); }
#define G_M(tt, ft, X, W) "v_mfma_f32_16x16x32_bf16 v[16*" S(tt) "+4*" S(ft) ":16*" S(tt) "+4*" S(ft) "+3], v[" S(W) "+4*" S(ft) ":" S(W) "+4*" S(ft) "+3], v[" S(X) "+4*" S(tt) ":" S(X) "+4*" S(tt) "+3], v[16*" S(tt) "+4*" S(ft) ":16*" S(tt) "+4*" S(ft) "+3]\n"
#define G_RX(tt, X, A) "ds_read_b128 v[" S(X) "+4*" S(tt) ":" S(X) "+4*" S(tt) "+3], %[" A "] offset:2048*" S(tt) "\n"
#define G_RW(ft, t, W, K) "ds_read_b64_tr_b16 v[" S(W) "+4*" S(ft) "+2*" S(t) ":" S(W) "+4*" S(ft) "+2*" S(t) "+1], %[aw" S(ft) "] offset:8192*" S(K) "+1024*" S(t) "\n"
#define G_READS(X, W, A, K) G_RX(0, X, A) G_RW(0, 0, W, K) G_RW(0, 1, W, K) G_RX(1, X, A) G_RW(1, 0, W, K) G_RW(1, 1, W, K) \
                            G_RX(2, X, A) G_RW(2, 0, W, K) G_RW(2, 1, W, K) G_RX(3, X, A) G_RW(3, 0, W, K) G_RW(3, 1, W, K)
#define G_HALF(X, W, X2, W2, A, K) \
  G_M(0, 0, X, W) G_RX(0, X2, A) G_M(0, 1, X, W) G_RW(0, 0, W2, K) G_M(0, 2, X, W) G_RW(0, 1, W2, K) G_M(0, 3, X, W) G_RX(1, X2, A) \
  G_M(1, 0, X, W) G_RW(1, 0, W2, K) G_M(1, 1, X, W) G_RW(1, 1, W2, K) G_M(1, 2, X, W) G_RX(2, X2, A) G_M(1, 3, X, W) G_RW(2, 0, W2, K) \
  G_M(2, 0, X, W) G_RW(2, 1, W2, K) G_M(2, 1, X, W) G_RX(3, X2, A) G_M(2, 2, X, W) G_RW(3, 0, W2, K) G_M(2, 3, X, W) G_RW(3, 1, W2, K)
#define G_LAST(X, W) G_M(3, 0, X, W) G_M(3, 1, X, W) G_M(3, 2, X, W) G_M(3, 3, X, W)

DI void touch(const char* a, unsigned& d) { asm volatile("global_load_dword %0, %1, off" : "+v"(d) : "v"(a)); }
DI void loader(const P& p, rsrc_t ws, int rb, int cg, int rj, int cj, int ch0, int nch, int lane) {
  {
    set_m0(L_PAR + 3072);
    if (lane < nch * 16) {
      const unsigned po = lane * 16;
      dma<-3072, false>(p.lar + ch0 * 256 + 3072, po); dma<-2304, false>(p.aim + ch0 * 256 + 2304, po);
      dma<-1536, false>(p.cre + ch0 * 256 + 1536, po); dma<-768, false>(p.cim + ch0 * 256 + 768, po);
      asm volatile("global_load_lds_ushort %0, %1" :: "v"(min(lane, nch - 1) * 2), "s"(p.ldt + ch0 * 2));
    }
    set_m0(L_MISC);
    if (lane < 2) asm volatile("global_load_lds_dword %0, %1 sc1" :: "v"(lane * 4), "s"(p.ws + F_E));
    const char* ub = p.u + rj * 64 * 1280 + cj * 160;
    unsigned vu[5];
#pragma unroll
    for (int i = 0; i < 5; i++) { const int c = i * 64 + lane; vu[i] = (c / 10) * 1280 + (c % 10) * 16 + 2048 - i * 1024; }
#pragma unroll
    for (int k = 0; k < 2; k++) {
      set_m0(L_U + k * 5120 + 2048);
      const char* s = ub + k * 32 * 1280;
      dma<-2048, false>(s, vu[0]); dma<-1024, false>(s, vu[1]); dma<0, false>(s, vu[2]); dma<1024, false>(s, vu[3]); dma<2048, false>(s, vu[4]);
    }
  }
  unsigned vx[8], vw[8];
  const unsigned r = lane >> 3, c = lane & 7, j = lane >> 4, q = (lane >> 2) & 3;
  const unsigned c0 = (unsigned)(0xee94dcba78653210ull >> (16 * j + 4 * (lane & 3))) & 15, c1 = (unsigned)(0xee94dcba78653210ull >> (16 * j + 4 * ((lane & 3) ^ 2))) & 15;
#pragma unroll
  for (int i = 0; i < 8; i++) {
    vx[i] = r * 1280 + ((c ^ (4 * (i & 1) + (r >> 1))) << 4) + i * (8 * 1280 - 1024);
    vw[i] = q * 3840 + ((i & 2) ? c1 : c0) * 16 + i * (4 * 3840 - 1024);
  }
  const char* xb = p.x + rb * 128 * 1280 + 4096;
  const char* wb = p.wqkv + cg * 240 + 4096;
  auto group = [&](const char* b, const unsigned (&v)[8], unsigned m) {
    set_m0(m + 4096);
    dma<-4096, false>(b, v[0]); dma<-3072, false>(b, v[1]); dma<-2048, false>(b, v[2]); dma<-1024, false>(b, v[3]);
    dma<0, false>(b, v[4]); dma<1024, false>(b, v[5]); dma<2048, false>(b, v[6]); dma<3072, false>(b, v[7]);
  };
  unsigned slot = 0;
#pragma unroll 1
  for (int s = -2; s < 10; s++) {
    if (s == 9) WAITV(0); else if (s >= -1) WAITV(32);
    if (s >= -1) bar();
    if (s < 8) {
      group(wb, vw, slot + 16384); group(xb, vx, slot); group(wb + 32 * 3840, vw, slot + 24576); group(xb + 64 * 1280, vx, slot + 8192);
      xb += 128; wb += 64 * 3840; slot = slot == 65536 ? 0 : slot + 32768;
    }
  }
  bar();
}

DI void gemm_phase(const P& p, rsrc_t ws, int rb, int cg, int rj, int cj, int wave, int lane) {
  const int wm = wave >> 1, wn = wave & 1, g = lane >> 4, tk = lane & 15;
  unsigned ax0 = (wm * 64 + tk) * 128 + ((g ^ (tk >> 1)) << 4), ax1 = ax0 ^ 64, aw[4];
#pragma unroll
  for (int f = 0; f < 4; f++) {
    const unsigned T = 4 * wn + f, n = lane & 3, q = (lane >> 2) & 3;
    const unsigned off = T < 6 ? 80 * (T >> 1) + 40 * (n >> 1) + 16 * (n & 1) + 8 * (T & 1) : 40 * (T == 6 ? n : 4 + (n & 1)) + 32;
    const unsigned v = (unsigned)(0x0eba98d6754c3210ull >> (4 * (off >> 4))) & 15;
    aw[f] = 16384 + g * 2048 + (16 * (v >> 2) + 4 * q + ((v & 3) ^ (2 * (g & 1)))) * 16 + (off & 8);
  }
  f32x16 acc[4];
#pragma unroll
  for (int a = 0; a < 4; a++)
#pragma unroll
    for (int i = 0; i < 16; i++) acc[a][i] = 0.f;
  bar();
#pragma unroll
  for (int i = 0; i < 3; i++) {
    const int t = wave + 4 * i;
    if (t < 10) {
      const int cb = t >> 1, oct = (t & 1) * 4 + g;
      const unsigned a = L_U + (oct * 8 + ((lane >> 2) & 3)) * 160 + cb * 32 + (lane & 3) * 8;
      st16(ws, WS_UT + (cj * 80 + cb * 16 + tk) * 4096 + (rj * 64 + oct * 8) * 2, cat(ldtr(a), ldtr(a + 4 * 160)));
    }
  }
  unsigned sink = 0;
  bar();
  asm volatile(
      "s_mov_b32 s42, 0\n"
      "s_mov_b32 s43, 0\n"
      G_READS(64, 80, "ax0", 0)
      "s_waitcnt lgkmcnt(0)\n"
      "1:\n"
      G_HALF(64, 80, 96, 112, "ax1", 1)
      "s_mov_b32 s44, 32768\n s_cmp_eq_u32 s43, 65536\n s_cselect_b32 s44, 0xffff0000, s44\n s_add_u32 s43, s43, s44\n"
      G_M(3, 0, 64, 80) "v_add_u32 %[ax0], s44, %[ax0]\n v_add_u32 %[ax1], s44, %[ax1]\n"
      G_M(3, 1, 64, 80) "v_add_u32 %[aw0], s44, %[aw0]\n v_add_u32 %[aw1], s44, %[aw1]\n"
      G_M(3, 2, 64, 80) "v_add_u32 %[aw2], s44, %[aw2]\n v_add_u32 %[aw3], s44, %[aw3]\n"
      "s_cmp_lg_u32 s42, 3\n"
      G_M(3, 3, 64, 80)
      "s_cbranch_scc1 4f\n s_waitcnt vmcnt(0)\n 4:\n"
      "s_waitcnt lgkmcnt(0)\n s_barrier\n"
      G_HALF(96, 112, 64, 80, "ax0", 0)
      "s_add_u32 s42, s42, 1\n s_cmp_lt_u32 s42, 10\n"
      G_LAST(96, 112)
      "s_waitcnt lgkmcnt(0)\n"
      "s_cbranch_scc1 1b\n"
      "s_nop 7\n"
      : "+{v[0:15]}"(acc[0]), "+{v[16:31]}"(acc[1]), "+{v[32:47]}"(acc[2]), "+{v[48:63]}"(acc[3]),
        [ax0] "+v"(ax0), [ax1] "+v"(ax1), [aw0] "+v"(aw[0]), [aw1] "+v"(aw[1]), [aw2] "+v"(aw[2]), [aw3] "+v"(aw[3]), "+v"(sink)
      :
      : "memory", "scc", "s42", "s43", "s44",
        "v64", "v65", "v66", "v67", "v68", "v69", "v70", "v71", "v72", "v73", "v74", "v75", "v76", "v77", "v78", "v79",
        "v80", "v81", "v82", "v83", "v84", "v85", "v86", "v87", "v88", "v89", "v90", "v91", "v92", "v93", "v94", "v95",
        "v96", "v97", "v98", "v99", "v100", "v101", "v102", "v103", "v104", "v105", "v106", "v107", "v108", "v109", "v110", "v111",
        "v112", "v113", "v114", "v115", "v116", "v117", "v118", "v119", "v120", "v121", "v122", "v123", "v124", "v125", "v126", "v127");
  STAMP(2);
  const float qs = 0.32259784f;
  auto image = [&](unsigned slot, unsigned t32) -> unsigned {
    const unsigned ty = slot >> 5, hd = slot & 31, pr = t32 >> 1;
    const unsigned kv = WS_KV + hd * 16 * POS + (pr & 15) * POS + (pr >> 4) * PAIR;
    return ty == 0 ? WS_Q + (hd * 64 + t32) * QT : ty == 1 ? kv + (t32 & 1) * 1024 : kv + 2560 + (t32 & 1) * 1536;
  };
  const unsigned t0 = rb * 4 + wm * 2;
#pragma unroll
  for (int fp = 0; fp < 2; fp++) {
    if (fp == 0 || wn == 0) {
      const unsigned slot = cg * 6 + 4 * wn + 2 * fp + (g >> 1), ty = slot >> 5;
      const float sc = ty == 0 ? qs : 1.f;
      const unsigned b0 = image(slot, t0), b1 = image(slot, t0 + 1);
      const unsigned lo = ty == 2 ? tk * 32 + (g & 1) * 16 : tk * 16 + (g & 1) * 512, st = ty == 2 ? 768 : 256;
#pragma unroll
      for (int tt = 0; tt < 4; tt++) {
        u32x4 v;
#pragma unroll
        for (int i = 0; i < 4; i++) v[i] = pk_bf16(acc[tt][8 * fp + 2 * i] * sc, acc[tt][8 * fp + 2 * i + 1] * sc);
        st16(ws, ((tt >> 1) ? b1 : b0) + lo + (tt & 1) * st, v);
      }
    } else {
#pragma unroll
      for (int f = 0; f < 2; f++) {
        const unsigned slot = cg * 6 + 4 * f + g, ty = slot >> 5;
        const float sc = ty == 0 ? qs : 1.f;
        if (f == 0 || g < 2) {
#pragma unroll
          for (int tt = 0; tt < 4; tt++) {
            u32x4 v; v[0] = pk_bf16(acc[tt][8 + 4 * f] * sc, acc[tt][9 + 4 * f] * sc); v[1] = pk_bf16(acc[tt][10 + 4 * f] * sc, acc[tt][11 + 4 * f] * sc);
            v[2] = 0x00003f80u; v[3] = 0;
            const unsigned b = image(slot, t0 + (tt >> 1));
            if (ty == 2) st16(ws, b + (tt & 1) * 768 + 512 + vtail_pos(tk >> 2) * 64 + (tk & 3) * 16, v);
            else { u32x2 w; w[0] = v[0]; w[1] = v[1];
              st8(ws, (ty == 0 ? b + 1024 : b + 2048 - (tt >> 1) * 768) + ((tt & 1) * 16 + tk) * 8, w); }
          }
        }
      }
    }
  }
}

DI unsigned wave_max_u32(unsigned m) {
  m = max(m, (unsigned)__builtin_amdgcn_update_dpp(0, (int)m, 0x111, 0xf, 0xf, true));
  m = max(m, (unsigned)__builtin_amdgcn_update_dpp(0, (int)m, 0x112, 0xf, 0xf, true));
  m = max(m, (unsigned)__builtin_amdgcn_update_dpp(0, (int)m, 0x114, 0xf, 0xf, true));
  m = max(m, (unsigned)__builtin_amdgcn_update_dpp(0, (int)m, 0x118, 0xf, 0xf, true));
  return max(max((unsigned)__builtin_amdgcn_readlane((int)m, 15), (unsigned)__builtin_amdgcn_readlane((int)m, 31)),
             max((unsigned)__builtin_amdgcn_readlane((int)m, 47), (unsigned)__builtin_amdgcn_readlane((int)m, 63)));
}
DI void stage_u(int c, int lane, const u32x4 (&v)[4]) {
  u16x2 m2 = {0, 0};
#pragma unroll
  for (int i = 0; i < 4; i++)
#pragma unroll
    for (int j = 0; j < 4; j++) m2 = __builtin_elementwise_max(m2, __builtin_bit_cast(u16x2, v[i][j] & 0x7fff7fffu));
  const int B = max((int)(wave_max_u32(max((unsigned)m2[0], (unsigned)m2[1])) >> 7) - 30, 0);
  u16x2 b2; b2[0] = b2[1] = (unsigned short)(B << 7);
  const unsigned ub = L_U + c * U_BYTES;
#pragma unroll
  for (int i = 0; i < 4; i++) {
    u32x4 w;
#pragma unroll
    for (int j = 0; j < 4; j++) {
      const u16x2 t = __builtin_elementwise_sub_sat(__builtin_bit_cast(u16x2, v[i][j] & 0x7fff7fffu), b2);
      w[j] = (__builtin_bit_cast(unsigned, t) << 3) | (v[i][j] & 0x80008000u);
    }
    ldsw<u32x4>(ub + 512 + i * 1024 + lane * 16, w);
  }
  if (lane < 32) ldsw<u32x4>(ub + lane * 16, u32x4{0, 0, 0, 0});
  if (lane == 0) ldsw<int>(L_MISC + 32 + c * 4, B);
}

#define KGEN_ASM \
  "v_mul_f32 v41, 0x41000000, %[ai]\nv_mul_f32 v37, 0x41000000, %[a2]\nv_mul_f32 v42, 0x43000000, %[ai]\nv_mul_f32 v38, 0x43000000, %[a2]\nv_mul_f32 v43, 0x43800000, %[ai]\n" \
  "v_mul_f32 v39, 0x43800000, %[a2]\nv_fract_f32 v40, %[ai]\nv_fract_f32 v41, v41\nv_fract_f32 v42, v42\nv_fract_f32 v43, v43\nv_exp_f32 v36, %[a2]\nv_exp_f32 v37, v37\n" \
  "v_exp_f32 v38, v38\nv_exp_f32 v39, v39\nv_cos_f32 v44, v40\nv_sin_f32 v48, v40\nv_cos_f32 v45, v41\nv_sin_f32 v49, v41\nv_cos_f32 v46, v42\nv_sin_f32 v50, v42\n" \
  "v_cos_f32 v47, v43\nv_sin_f32 v51, v43\nv_mov_b32 v52, 0x3c00\nv_mul_f32 v0, v36, v44\nv_mul_f32 v1, v36, v48\nv_mul_f32 v2, v36, -v48\nv_mul_f32 v3, v37, v45\n" \
  "v_mul_f32 v4, v37, v49\nv_mul_f32 v5, v37, -v49\nv_mul_f32 v20, v38, v46\nv_mul_f32 v21, v38, v50\nv_mul_f32 v6, v39, v47\nv_mul_f32 v7, v39, v51\n" \
  "v_mul_f32 v8, v39, -v51\nv_mul_f32 v9, v1, v2\nv_mul_f32 v10, v1, v0\nv_mul_f32 v28, %[ci], v8\nv_mul_f32 v29, %[ci], v6\nv_mul_f32 v26, %[ci], v2\n" \
  "v_mul_f32 v27, %[ci], v0\nv_cvt_pk_f16_f32 v53, v20, v21\nv_cvt_pk_f16_f32 v54, %[cr], -%[ci]\nv_fmac_f32 v9, v0, v0\nv_fmac_f32 v10, v0, v1\nv_fmac_f32 v28, %[cr], v6\n" \
  "v_fmac_f32 v29, %[cr], v7\nv_fmac_f32 v26, %[cr], v0\nv_fmac_f32 v27, %[cr], v1\nds_write2st64_b32 v64, v52, v53 offset0:32 offset1:48\nv_xor_b32 v11, 0x80000000, v10\n" \
  "v_mul_f32 v22, v21, v5\nv_mul_f32 v23, v21, v3\nv_mul_f32 v12, v10, v11\nv_mul_f32 v13, v10, v9\nv_mul_f32 v32, v29, v8\nv_mul_f32 v33, v29, v6\nv_mul_f32 v30, v29, v11\n" \
  "v_mul_f32 v31, v29, v9\nv_cvt_pk_f16_f32 v55, v28, -v29\nv_fmac_f32 v22, v20, v3\nv_fmac_f32 v23, v20, v4\nv_fmac_f32 v12, v9, v9\nv_fmac_f32 v13, v9, v10\n" \
  "v_fmac_f32 v32, v28, v6\nv_fmac_f32 v33, v28, v7\nv_fmac_f32 v30, v28, v9\nv_fmac_f32 v31, v28, v10\nds_write2st64_b32 v64, v54, v55 offset0:0 offset1:16\n" \
  "v_xor_b32 v14, 0x80000000, v13\nv_mul_f32 v16, v4, v5\nv_mul_f32 v17, v4, v3\nv_mul_f32 v20, v23, v5\nv_mul_f32 v21, v23, v3\nv_mul_f32 v24, v27, v2\n" \
  "v_mul_f32 v25, v27, v0\nv_mul_f32 v28, v31, v11\nv_mul_f32 v29, v31, v9\nv_cvt_pk_f16_f32 v52, v3, v4\nv_cvt_pk_f16_f32 v53, v22, v23\nv_cvt_pk_f16_f32 v54, v26, -v27\n" \
  "v_cvt_pk_f16_f32 v55, v30, -v31\nv_fmac_f32 v16, v3, v3\nv_fmac_f32 v17, v3, v4\nv_fmac_f32 v20, v22, v3\nv_fmac_f32 v21, v22, v4\nv_fmac_f32 v24, v26, v0\n" \
  "v_fmac_f32 v25, v26, v1\nv_fmac_f32 v28, v30, v9\nv_fmac_f32 v29, v30, v10\nds_write2st64_b32 v65, v52, v53 offset0:33 offset1:49\n" \
  "ds_write2st64_b32 v65, v54, v55 offset0:1 offset1:17\ns_barrier\nv_mul_f32 v18, v17, v5\nv_mul_f32 v19, v17, v3\nv_mul_f32 v22, v21, v5\nv_mul_f32 v23, v21, v3\n" \
  "v_mul_f32 v26, v25, v2\nv_mul_f32 v27, v25, v0\nv_mul_f32 v30, v29, v11\nv_mul_f32 v31, v29, v9\nv_cvt_pk_f16_f32 v52, v16, v17\nv_cvt_pk_f16_f32 v53, v20, v21\n" \
  "v_cvt_pk_f16_f32 v54, v24, -v25\nv_cvt_pk_f16_f32 v55, v28, -v29\nv_fmac_f32 v18, v16, v3\nv_fmac_f32 v19, v16, v4\nv_fmac_f32 v22, v20, v3\nv_fmac_f32 v23, v20, v4\n" \
  "v_fmac_f32 v26, v24, v0\nv_fmac_f32 v27, v24, v1\nv_fmac_f32 v30, v28, v9\nv_fmac_f32 v31, v28, v10\nds_write2st64_b32 v66, v52, v53 offset0:34 offset1:50\n" \
  "ds_write2st64_b32 v66, v54, v55 offset0:2 offset1:18\nv_mul_f32 v16, v19, v5\nv_mul_f32 v17, v19, v3\nv_mul_f32 v20, v23, v5\nv_mul_f32 v21, v23, v3\n" \
  "v_mul_f32 v24, v27, v2\nv_mul_f32 v25, v27, v0\nv_cvt_pk_f16_f32 v52, v18, v19\nv_cvt_pk_f16_f32 v53, v22, v23\nv_cvt_pk_f16_f32 v54, v26, -v27\n" \
  "v_cvt_pk_f16_f32 v55, v30, -v31\nv_fmac_f32 v16, v18, v3\nv_fmac_f32 v17, v18, v4\nv_fmac_f32 v20, v22, v3\nv_fmac_f32 v21, v22, v4\nv_fmac_f32 v24, v26, v0\n" \
  "v_fmac_f32 v25, v26, v1\nds_write2st64_b32 v67, v52, v53 offset0:35 offset1:51\nds_write2st64_b32 v67, v54, v55 offset0:3 offset1:19\nv_mul_f32 v18, v17, v5\n" \
  "v_mul_f32 v19, v17, v3\nv_mul_f32 v22, v21, v5\nv_mul_f32 v23, v21, v3\nv_mul_f32 v26, v25, v2\nv_mul_f32 v27, v25, v0\nv_mul_f32 v28, v33, v14\nv_mul_f32 v29, v33, v12\n" \
  "v_cvt_pk_f16_f32 v52, v16, v17\nv_cvt_pk_f16_f32 v53, v20, v21\nv_cvt_pk_f16_f32 v54, v24, -v25\nv_cvt_pk_f16_f32 v55, v32, -v33\nv_fmac_f32 v18, v16, v3\n" \
  "v_fmac_f32 v19, v16, v4\nv_fmac_f32 v22, v20, v3\nv_fmac_f32 v23, v20, v4\nv_fmac_f32 v26, v24, v0\nv_fmac_f32 v27, v24, v1\nv_fmac_f32 v28, v32, v12\n" \
  "v_fmac_f32 v29, v32, v13\nds_write2st64_b32 v68, v52, v53 offset0:36 offset1:52\nds_write2st64_b32 v68, v54, v55 offset0:4 offset1:20\ns_barrier\nv_mul_f32 v16, v19, v5\n" \
  "v_mul_f32 v17, v19, v3\nv_mul_f32 v20, v23, v5\nv_mul_f32 v21, v23, v3\nv_mul_f32 v24, v27, v2\nv_mul_f32 v25, v27, v0\nv_mul_f32 v34, v33, v8\nv_mul_f32 v35, v33, v6\n" \
  "v_cvt_pk_f16_f32 v52, v18, v19\nv_cvt_pk_f16_f32 v53, v22, v23\nv_cvt_pk_f16_f32 v54, v26, -v27\nv_cvt_pk_f16_f32 v55, v28, -v29\nv_fmac_f32 v16, v18, v3\n" \
  "v_fmac_f32 v17, v18, v4\nv_fmac_f32 v20, v22, v3\nv_fmac_f32 v21, v22, v4\nv_fmac_f32 v24, v26, v0\nv_fmac_f32 v25, v26, v1\nv_fmac_f32 v34, v32, v6\n" \
  "v_fmac_f32 v35, v32, v7\nds_write2st64_b32 v69, v52, v53 offset0:37 offset1:53\nds_write2st64_b32 v69, v54, v55 offset0:5 offset1:21\nv_mul_f32 v18, v17, v5\n" \
  "v_mul_f32 v19, v17, v3\nv_mul_f32 v22, v21, v5\nv_mul_f32 v23, v21, v3\nv_mul_f32 v26, v25, v2\nv_mul_f32 v27, v25, v0\nv_mul_f32 v28, v35, v14\nv_mul_f32 v29, v35, v12\n" \
  "v_cvt_pk_f16_f32 v52, v16, v17\nv_cvt_pk_f16_f32 v53, v20, v21\nv_cvt_pk_f16_f32 v54, v24, -v25\nv_cvt_pk_f16_f32 v55, v34, -v35\nv_fmac_f32 v18, v16, v3\n" \
  "v_fmac_f32 v19, v16, v4\nv_fmac_f32 v22, v20, v3\nv_fmac_f32 v23, v20, v4\nv_fmac_f32 v26, v24, v0\nv_fmac_f32 v27, v24, v1\nv_fmac_f32 v28, v34, v12\n" \
  "v_fmac_f32 v29, v34, v13\nds_write2st64_b32 v70, v52, v53 offset0:38 offset1:54\nds_write2st64_b32 v70, v54, v55 offset0:6 offset1:22\nv_mul_f32 v16, v19, v5\n" \
  "v_mul_f32 v17, v19, v3\nv_mul_f32 v20, v23, v5\nv_mul_f32 v21, v23, v3\nv_mul_f32 v32, v35, v8\nv_mul_f32 v33, v35, v6\nv_cvt_pk_f16_f32 v52, v18, v19\n" \
  "v_cvt_pk_f16_f32 v53, v22, v23\nv_cvt_pk_f16_f32 v54, v26, -v27\nv_cvt_pk_f16_f32 v55, v28, -v29\nv_fmac_f32 v16, v18, v3\nv_fmac_f32 v17, v18, v4\n" \
  "v_fmac_f32 v20, v22, v3\nv_fmac_f32 v21, v22, v4\nv_fmac_f32 v32, v34, v6\nv_fmac_f32 v33, v34, v7\nds_write2st64_b32 v71, v52, v53 offset0:39 offset1:55\n" \
  "ds_write2st64_b32 v71, v54, v55 offset0:7 offset1:23\nv_mul_f32 v18, v17, v5\nv_mul_f32 v19, v17, v3\nv_mul_f32 v22, v21, v5\nv_mul_f32 v23, v21, v3\n" \
  "v_mul_f32 v34, v33, v8\nv_mul_f32 v35, v33, v6\nv_cvt_pk_f16_f32 v52, v16, v17\nv_cvt_pk_f16_f32 v53, v20, v21\nv_cvt_pk_f16_f32 v56, v32, -v33\nv_fmac_f32 v18, v16, v3\n" \
  "v_fmac_f32 v19, v16, v4\nv_fmac_f32 v22, v20, v3\nv_fmac_f32 v23, v20, v4\nv_fmac_f32 v34, v32, v6\nv_fmac_f32 v35, v32, v7\n" \
  "ds_write2st64_b32 v72, v52, v53 offset0:40 offset1:56\ns_barrier\nv_mul_f32 v16, v19, v5\nv_mul_f32 v17, v19, v3\nv_mul_f32 v20, v23, v5\nv_mul_f32 v21, v23, v3\n" \
  "v_mul_f32 v32, v35, v8\nv_mul_f32 v33, v35, v6\nv_cvt_pk_f16_f32 v52, v18, v19\nv_cvt_pk_f16_f32 v53, v22, v23\nv_cvt_pk_f16_f32 v57, v34, -v35\nv_fmac_f32 v16, v18, v3\n" \
  "v_fmac_f32 v17, v18, v4\nv_fmac_f32 v20, v22, v3\nv_fmac_f32 v21, v22, v4\nv_fmac_f32 v32, v34, v6\nv_fmac_f32 v33, v34, v7\n" \
  "ds_write2st64_b32 v73, v52, v53 offset0:41 offset1:57\nv_mul_f32 v18, v17, v5\nv_mul_f32 v19, v17, v3\nv_mul_f32 v22, v21, v5\nv_mul_f32 v23, v21, v3\n" \
  "v_mul_f32 v34, v33, v8\nv_mul_f32 v35, v33, v6\nv_cvt_pk_f16_f32 v52, v16, v17\nv_cvt_pk_f16_f32 v53, v20, v21\nv_cvt_pk_f16_f32 v54, v32, -v33\nv_fmac_f32 v18, v16, v3\n" \
  "v_fmac_f32 v19, v16, v4\nv_fmac_f32 v22, v20, v3\nv_fmac_f32 v23, v20, v4\nv_fmac_f32 v34, v32, v6\nv_fmac_f32 v35, v32, v7\n" \
  "ds_write2st64_b32 v74, v52, v53 offset0:42 offset1:58\nds_write_b32 v74, v54 offset:2560\nv_mul_f32 v16, v19, v5\nv_mul_f32 v17, v19, v3\nv_mul_f32 v20, v23, v5\n" \
  "v_mul_f32 v21, v23, v3\nv_mul_f32 v32, v35, v8\nv_mul_f32 v33, v35, v6\nv_cvt_pk_f16_f32 v52, v18, v19\nv_cvt_pk_f16_f32 v53, v22, v23\nv_cvt_pk_f16_f32 v55, v34, -v35\n" \
  "v_fmac_f32 v16, v18, v3\nv_fmac_f32 v17, v18, v4\nv_fmac_f32 v20, v22, v3\nv_fmac_f32 v21, v22, v4\nv_fmac_f32 v32, v34, v6\nv_fmac_f32 v33, v34, v7\n" \
  "ds_write2st64_b32 v75, v52, v53 offset0:43 offset1:59\nds_write2st64_b32 v72, v56, v55 offset0:8 offset1:24\nv_mul_f32 v18, v17, v5\nv_mul_f32 v19, v17, v3\n" \
  "v_mul_f32 v22, v21, v5\nv_mul_f32 v23, v21, v3\nv_cvt_pk_f16_f32 v52, v16, v17\nv_cvt_pk_f16_f32 v53, v20, v21\nv_cvt_pk_f16_f32 v55, v32, -v33\nv_fmac_f32 v18, v16, v3\n" \
  "v_fmac_f32 v19, v16, v4\nv_fmac_f32 v22, v20, v3\nv_fmac_f32 v23, v20, v4\nds_write2st64_b32 v76, v52, v53 offset0:44 offset1:60\n" \
  "ds_write2st64_b32 v73, v57, v55 offset0:9 offset1:25\nv_mul_f32 v16, v19, v5\nv_mul_f32 v17, v19, v3\nv_mul_f32 v20, v23, v5\nv_mul_f32 v21, v23, v3\n" \
  "v_cvt_pk_f16_f32 v52, v18, v19\nv_cvt_pk_f16_f32 v53, v22, v23\nv_fmac_f32 v16, v18, v3\nv_fmac_f32 v17, v18, v4\nv_fmac_f32 v20, v22, v3\nv_fmac_f32 v21, v22, v4\n" \
  "ds_write2st64_b32 v77, v52, v53 offset0:45 offset1:61\ns_barrier\nv_mul_f32 v18, v17, v5\nv_mul_f32 v19, v17, v3\nv_mul_f32 v22, v21, v5\nv_mul_f32 v23, v21, v3\n" \
  "v_cvt_pk_f16_f32 v52, v16, v17\nv_cvt_pk_f16_f32 v53, v20, v21\nv_fmac_f32 v18, v16, v3\nv_fmac_f32 v19, v16, v4\nv_fmac_f32 v22, v20, v3\nv_fmac_f32 v23, v20, v4\n" \
  "ds_write2st64_b32 v78, v52, v53 offset0:46 offset1:62\nv_cvt_pk_f16_f32 v52, v18, v19\nv_cvt_pk_f16_f32 v53, v22, v23\n" \
  "ds_write2st64_b32 v79, v52, v53 offset0:47 offset1:63\nds_read_b128 v[0:3], v80\nds_read_b128 v[4:7], v80 offset:8192\nds_read_b128 v[8:11], v81\n" \
  "ds_read_b128 v[12:15], v81 offset:8192\nds_read_b128 v[16:19], v82\nds_read_b128 v[20:23], v82 offset:8192\nds_read_b128 v[24:27], v83\n" \
  "ds_read_b128 v[28:31], v83 offset:8192\nds_read_b128 v[32:35], v84\nds_read_b128 v[36:39], v84 offset:8192\nds_read_b128 v[40:43], v85\n" \
  "ds_read_b128 v[44:47], v85 offset:8192\nds_read_b128 v[48:51], v86\nds_read_b128 v[52:55], v86 offset:8192\nds_read_b128 v[56:59], v87\n" \
  "ds_read_b128 v[60:63], v87 offset:8192\ns_waitcnt lgkmcnt(14)\nv_mfma_f32_32x32x16_f16 %[acc], v[0:3], v[4:7], %[acc]\ns_waitcnt lgkmcnt(12)\n" \
  "v_mfma_f32_32x32x16_f16 %[acc], v[8:11], v[12:15], %[acc]\ns_waitcnt lgkmcnt(10)\nv_mfma_f32_32x32x16_f16 %[acc], v[16:19], v[20:23], %[acc]\ns_waitcnt lgkmcnt(8)\n" \
  "v_mfma_f32_32x32x16_f16 %[acc], v[24:27], v[28:31], %[acc]\ns_waitcnt lgkmcnt(6)\nv_mfma_f32_32x32x16_f16 %[acc], v[32:35], v[36:39], %[acc]\ns_waitcnt lgkmcnt(4)\n" \
  "v_mfma_f32_32x32x16_f16 %[acc], v[40:43], v[44:47], %[acc]\ns_waitcnt lgkmcnt(2)\nv_mfma_f32_32x32x16_f16 %[acc], v[48:51], v[52:55], %[acc]\ns_waitcnt lgkmcnt(0)\n" \
  "v_mfma_f32_32x32x16_f16 %[acc], v[56:59], v[60:63], %[acc]\ns_barrier\ns_nop 10\n"
typedef unsigned u32x16 __attribute__((ext_vector_type(16)));
typedef unsigned u32x8 __attribute__((ext_vector_type(8)));
#define CLOB64 "v0", "v1", "v2", "v3", "v4", "v5", "v6", "v7", "v8", "v9", "v10", "v11", "v12", "v13", "v14", "v15", "v16", "v17", "v18", "v19", "v20", "v21", \
  "v22", "v23", "v24", "v25", "v26", "v27", "v28", "v29", "v30", "v31", "v32", "v33", "v34", "v35", "v36", "v37", "v38", "v39", "v40", "v41", "v42", "v43", \
  "v44", "v45", "v46", "v47", "v48", "v49", "v50", "v51", "v52", "v53", "v54", "v55", "v56", "v57", "v58", "v59", "v60", "v61", "v62", "v63"
DI void kgen(const P& p, rsrc_t ws, int ch, int c, unsigned fu_mine, int lane) {
  const unsigned scr = L_W4 + c * W4_BYTES, kc = L_KC + c * 2048;
  const int r = lane & 31, h = lane >> 5;
  u32x16 aw; u32x8 ar;
#pragma unroll
  for (int i = 0; i < 16; i++) aw[i] = scr + ((lane * 4) ^ (i << 4));
#pragma unroll
  for (int k = 0; k < 8; k++) ar[k] = scr + r * 256 + (((2 * k + h) ^ (r & 15)) << 4);
  STAMP(20); bar(); STAMP(21);
  const unsigned pa = L_PAR + c * 256 + lane * 4;
  const unsigned rl = ldsr<unsigned>(pa), ri = ldsr<unsigned>(pa + 768), rc = ldsr<unsigned>(pa + 1536), rd = ldsr<unsigned>(pa + 2304);
  const unsigned short ldt = ldsr<unsigned short>(L_PAR + 3072 + c * 4);
  const float dt = rbf(expf(bf2f(ldt)));
  auto lo = [](unsigned v) { return __uint_as_float(v << 16); };
  auto hi = [](unsigned v) { return __uint_as_float(v & 0xffff0000u); };
  float a2[2] = {-rbf(expf(lo(rl))) * dt * 1.44269504f, -rbf(expf(hi(rl))) * dt * 1.44269504f};
  float ai[2] = {lo(ri) * dt * 0.15915494f, hi(ri) * dt * 0.15915494f};
  float cr[2] = {lo(rc) * dt * 2.f, hi(rc) * dt * 2.f}, ci[2] = {lo(rd) * dt * 2.f, hi(rd) * dt * 2.f};
  const float mc = __uint_as_float(wave_max_u32(__float_as_uint(fmaxf(fmaxf(fabsf(cr[0]), fabsf(ci[0])), fmaxf(fabsf(cr[1]), fabsf(ci[1]))))));
  const int e = (mc > 0.f && mc < 3e38f) ? 8 - __builtin_amdgcn_frexp_expf(mc) : 0;
  if (lane == 0) ldsw<int>(L_MISC + 16 + c * 4, e);
#pragma unroll
  for (int s = 0; s < 2; s++) { cr[s] = ldexpf(cr[s], e); ci[s] = ldexpf(ci[s], e); }
  f32x16 acc;
#pragma unroll
  for (int i = 0; i < 16; i++) acc[i] = 0.f;
  const u32x2 tag = call_tag();
  STAMP(22);
#pragma unroll 1
  for (int s = 0; s < 2; s++) {
    asm volatile(KGEN_ASM : [acc] "+v"(acc) : [a2] "v"(a2[0]), [ai] "v"(ai[0]), [cr] "v"(cr[0]), [ci] "v"(ci[0]), "{v[64:79]}"(aw), "{v[80:87]}"(ar) : "memory", CLOB64);
    if (s == 0) { STAMP(28); if (c == 0 && lane == 0) st8(ws, fu_mine, tag); }
    a2[0] = a2[1]; ai[0] = ai[1]; cr[0] = cr[1]; ci[0] = ci[1];
  }
  STAMP(30);
  const unsigned fo = F_U + (ch / 80 * 32 + r) * 8;
  const u32x2 f = ld8(ws, fo);
  ldsw<u32x2>(kc + (8 * r + 4 * h) * 2, u32x2{pk_f16(acc[0], acc[1]), pk_f16(acc[2], acc[3])});
  ldsw<unsigned>(kc + (h ? 384 + 2 * r : 256 + 4 * r) * 2, pk_f16(acc[8], acc[9]));
  ldsw<unsigned>(kc + (h ? 448 + 2 * r : 258 + 4 * r) * 2, pk_f16(acc[10], acc[11]));
  if (!h) {
#pragma unroll
    for (int i = 0; i < 3; i++) ldsw<_Float16>(kc + (512 + 32 * i + r) * 2, (_Float16)acc[4 + i]);
    ldsw<_Float16>(kc + (608 + r) * 2, (_Float16)acc[12]); ldsw<_Float16>(kc + (640 + r) * 2, (_Float16)acc[13]);
  } else ldsw<unsigned short>(kc + 672 * 2, 0);
  bar();
  if (__builtin_amdgcn_ballot_w64(f[0] != tag[0] || f[1] != tag[1]) != 0) tags_wait(ws, fo, tag);
  asm volatile("" ::: "memory");
}
DI void windows(int t, int nch) {
#pragma unroll 1
  for (int it = 0; it < 2; it++) {
    unsigned a[7];
#pragma unroll
    for (int i = 0; i < 7; i++) {
      const int x = 1024 * it + 4 * t - 3 + i;
      const int far = 2 * (int)(pk_bf16((float)x, 0.f) & 0xffffu) - 2 * (0x4300 - 128);
      a[i] = L_KC + (x < 0 ? 672 * 2 : x < 128 ? 2 * x : far);
    }
    const unsigned w = L_W4 + 128 + it * 8192 + t * 32;
#pragma unroll
    for (int c = 0; c < 3; c++) {
      if (c < nch) {
        unsigned k[7];
#pragma unroll
        for (int i = 0; i < 7; i++) k[i] = ldsr<unsigned short>(a[i] + c * 2048);
        auto D = [&](int y) { return k[y + 3] | (k[y + 2] << 16); };
        ldsw<u32x4>(w + c * W4_BYTES, u32x4{D(0), D(-2), D(1), D(-1)});
        ldsw<u32x4>(w + c * W4_BYTES + 16, u32x4{D(2), D(0), D(3), D(1)});
      }
    }
  }
  if (t < 24) ldsw<u32x4>(L_W4 + (t >> 3) * W4_BYTES + (t & 7) * 16, u32x4{0, 0, 0, 0});
}

#define CONV_ASM0 \
  "ds_read_b64 v[0:1], %[aA] offset:32\nds_read_b64 v[2:3], %[aA] offset:0\nds_read_b128 v[60:63], %[aB] offset:3712\nds_read_b128 v[48:51], %[aB] offset:2176\n" \
  "ds_read_b128 v[44:47], %[aB] offset:1664\nds_read_b128 v[32:35], %[aB] offset:128\nds_read_b64 v[4:5], %[aA] offset:2080\nds_read_b64 v[6:7], %[aA] offset:2048\n" \
  "ds_read_b128 v[56:59], %[aB] offset:3200\nds_read_b64 v[8:9], %[aA] offset:4128\nds_read_b64 v[10:11], %[aA] offset:4096\nds_read_b128 v[52:55], %[aB] offset:2688\n" \
  "ds_read_b64 v[12:13], %[aA] offset:6176\nds_read_b64 v[14:15], %[aA] offset:6144\nds_read_b128 v[40:43], %[aB] offset:1152\nds_read_b64 v[16:17], %[aA] offset:8224\n" \
  "ds_read_b64 v[18:19], %[aA] offset:8192\nds_read_b128 v[36:39], %[aB] offset:640\nds_read_b64 v[20:21], %[aA] offset:10272\nds_read_b64 v[22:23], %[aA] offset:10240\n" \
  "ds_read_b64 v[24:25], %[aA] offset:12320\nds_read_b64 v[26:27], %[aA] offset:12288\nds_read_b64 v[28:29], %[aA] offset:14368\nds_read_b64 v[30:31], %[aA] offset:14336\n" \
  "s_waitcnt lgkmcnt(0)\n1:\nv_mfma_f32_16x16x32_f16 %[c3], v[0:3], v[60:63], %[c3]\nds_read_b64 v[64:65], %[aA] offset:288\nds_read_b64 v[66:67], %[aA] offset:256\n" \
  "v_mfma_f32_16x16x32_f16 %[c2], v[0:3], v[48:51], %[c2]\nds_read_b128 v[124:127], %[aB] offset:3648\nds_read_b128 v[112:115], %[aB] offset:2112\n" \
  "v_mfma_f32_16x16x32_f16 %[c1], v[0:3], v[44:47], %[c1]\nds_read_b128 v[108:111], %[aB] offset:1600\nds_read_b128 v[96:99], %[aB] offset:64\n" \
  "v_mfma_f32_16x16x32_f16 %[c0], v[0:3], v[32:35], %[c0]\nds_read_b64 v[68:69], %[aA] offset:2336\nds_read_b64 v[70:71], %[aA] offset:2304\n" \
  "v_mfma_f32_16x16x32_f16 %[c3], v[4:7], v[56:59], %[c3]\nds_read_b128 v[120:123], %[aB] offset:3136\nds_read_b64 v[72:73], %[aA] offset:4384\n" \
  "v_mfma_f32_16x16x32_f16 %[c2], v[4:7], v[44:47], %[c2]\nds_read_b64 v[74:75], %[aA] offset:4352\nds_read_b128 v[116:119], %[aB] offset:2624\n" \
  "v_mfma_f32_16x16x32_f16 %[c1], v[4:7], v[40:43], %[c1]\nds_read_b64 v[76:77], %[aA] offset:6432\nds_read_b64 v[78:79], %[aA] offset:6400\n" \
  "v_mfma_f32_16x16x32_f16 %[c3], v[8:11], v[52:55], %[c3]\nds_read_b128 v[104:107], %[aB] offset:1088\nds_read_b64 v[80:81], %[aA] offset:8480\n" \
  "v_mfma_f32_16x16x32_f16 %[c2], v[8:11], v[40:43], %[c2]\nds_read_b64 v[82:83], %[aA] offset:8448\nds_read_b128 v[100:103], %[aB] offset:576\n" \
  "v_mfma_f32_16x16x32_f16 %[c1], v[8:11], v[36:39], %[c1]\nds_read_b64 v[84:85], %[aA] offset:10528\nv_mfma_f32_16x16x32_f16 %[c3], v[12:15], v[48:51], %[c3]\n" \
  "ds_read_b64 v[86:87], %[aA] offset:10496\nv_mfma_f32_16x16x32_f16 %[c2], v[12:15], v[36:39], %[c2]\nds_read_b64 v[88:89], %[aA] offset:12576\n" \
  "v_mfma_f32_16x16x32_f16 %[c1], v[12:15], v[32:35], %[c1]\nds_read_b64 v[90:91], %[aA] offset:12544\nv_mfma_f32_16x16x32_f16 %[c3], v[16:19], v[44:47], %[c3]\n" \
  "ds_read_b64 v[92:93], %[aA] offset:14624\nv_mfma_f32_16x16x32_f16 %[c2], v[16:19], v[32:35], %[c2]\nds_read_b64 v[94:95], %[aA] offset:14592\n" \
  "v_mfma_f32_16x16x32_f16 %[c3], v[20:23], v[40:43], %[c3]\nv_mfma_f32_16x16x32_f16 %[c3], v[24:27], v[36:39], %[c3]\n" \
  "v_mfma_f32_16x16x32_f16 %[c3], v[28:31], v[32:35], %[c3]\ns_sub_u32 %[n], %[n], 1\ns_cmp_eq_u32 %[n], 0\ns_waitcnt lgkmcnt(0)\ns_cbranch_scc1 2f\n" \
  "v_mfma_f32_16x16x32_f16 %[c3], v[64:67], v[124:127], %[c3]\nds_read_b64 v[0:1], %[aA] offset:544\nds_read_b64 v[2:3], %[aA] offset:512\n" \
  "v_mfma_f32_16x16x32_f16 %[c2], v[64:67], v[112:115], %[c2]\nds_read_b128 v[60:63], %[aB] offset:3584\nds_read_b128 v[48:51], %[aB] offset:2048\n" \
  "v_mfma_f32_16x16x32_f16 %[c1], v[64:67], v[108:111], %[c1]\nds_read_b128 v[44:47], %[aB] offset:1536\nds_read_b128 v[32:35], %[aB] offset:0\n" \
  "v_mfma_f32_16x16x32_f16 %[c0], v[64:67], v[96:99], %[c0]\nds_read_b64 v[4:5], %[aA] offset:2592\nds_read_b64 v[6:7], %[aA] offset:2560\n" \
  "v_mfma_f32_16x16x32_f16 %[c3], v[68:71], v[120:123], %[c3]\nds_read_b128 v[56:59], %[aB] offset:3072\nds_read_b64 v[8:9], %[aA] offset:4640\n" \
  "v_mfma_f32_16x16x32_f16 %[c2], v[68:71], v[108:111], %[c2]\nds_read_b64 v[10:11], %[aA] offset:4608\nds_read_b128 v[52:55], %[aB] offset:2560\n" \
  "v_mfma_f32_16x16x32_f16 %[c1], v[68:71], v[104:107], %[c1]\nds_read_b64 v[12:13], %[aA] offset:6688\nds_read_b64 v[14:15], %[aA] offset:6656\n" \
  "v_mfma_f32_16x16x32_f16 %[c3], v[72:75], v[116:119], %[c3]\nds_read_b128 v[40:43], %[aB] offset:1024\nds_read_b64 v[16:17], %[aA] offset:8736\n" \
  "v_mfma_f32_16x16x32_f16 %[c2], v[72:75], v[104:107], %[c2]\nds_read_b64 v[18:19], %[aA] offset:8704\nds_read_b128 v[36:39], %[aB] offset:512\n" \
  "v_mfma_f32_16x16x32_f16 %[c1], v[72:75], v[100:103], %[c1]\nds_read_b64 v[20:21], %[aA] offset:10784\nv_mfma_f32_16x16x32_f16 %[c3], v[76:79], v[112:115], %[c3]\n" \
  "ds_read_b64 v[22:23], %[aA] offset:10752\nv_mfma_f32_16x16x32_f16 %[c2], v[76:79], v[100:103], %[c2]\nds_read_b64 v[24:25], %[aA] offset:12832\n" \
  "v_mfma_f32_16x16x32_f16 %[c1], v[76:79], v[96:99], %[c1]\nds_read_b64 v[26:27], %[aA] offset:12800\nv_mfma_f32_16x16x32_f16 %[c3], v[80:83], v[108:111], %[c3]\n" \
  "ds_read_b64 v[28:29], %[aA] offset:14880\nv_mfma_f32_16x16x32_f16 %[c2], v[80:83], v[96:99], %[c2]\nds_read_b64 v[30:31], %[aA] offset:14848\n" \
  "v_mfma_f32_16x16x32_f16 %[c3], v[84:87], v[104:107], %[c3]\nv_mfma_f32_16x16x32_f16 %[c3], v[88:91], v[100:103], %[c3]\n" \
  "v_mfma_f32_16x16x32_f16 %[c3], v[92:95], v[96:99], %[c3]\nv_add_u32 %[aA], 0x200, %[aA]\nv_subrev_u32 %[aB], 0x80, %[aB]\ns_cmp_lg_u32 %[wv], 0\n" \
  "s_waitcnt vmcnt(0) lgkmcnt(0)\ns_barrier\ns_cbranch_scc1 1b\ns_mov_b64 exec, 1\nbuffer_store_dwordx2 %[tag], off, %[ws], %[fo] sc1\ns_mov_b64 exec, -1\ns_branch 1b\n2:\n" \
  "v_mfma_f32_16x16x32_f16 %[c3], v[64:67], v[124:127], %[c3]\nv_mfma_f32_16x16x32_f16 %[c2], v[64:67], v[112:115], %[c2]\n" \
  "v_mfma_f32_16x16x32_f16 %[c1], v[64:67], v[108:111], %[c1]\nv_mfma_f32_16x16x32_f16 %[c0], v[64:67], v[96:99], %[c0]\n" \
  "v_mfma_f32_16x16x32_f16 %[c3], v[68:71], v[120:123], %[c3]\nv_mfma_f32_16x16x32_f16 %[c2], v[68:71], v[108:111], %[c2]\n" \
  "v_mfma_f32_16x16x32_f16 %[c1], v[68:71], v[104:107], %[c1]\nv_mfma_f32_16x16x32_f16 %[c3], v[72:75], v[116:119], %[c3]\n" \
  "v_mfma_f32_16x16x32_f16 %[c2], v[72:75], v[104:107], %[c2]\nv_mfma_f32_16x16x32_f16 %[c1], v[72:75], v[100:103], %[c1]\n" \
  "v_mfma_f32_16x16x32_f16 %[c3], v[76:79], v[112:115], %[c3]\nv_mfma_f32_16x16x32_f16 %[c2], v[76:79], v[100:103], %[c2]\n" \
  "v_mfma_f32_16x16x32_f16 %[c1], v[76:79], v[96:99], %[c1]\nv_mfma_f32_16x16x32_f16 %[c3], v[80:83], v[108:111], %[c3]\n" \
  "v_mfma_f32_16x16x32_f16 %[c2], v[80:83], v[96:99], %[c2]\nv_mfma_f32_16x16x32_f16 %[c3], v[84:87], v[104:107], %[c3]\n" \
  "v_mfma_f32_16x16x32_f16 %[c3], v[88:91], v[100:103], %[c3]\nv_mfma_f32_16x16x32_f16 %[c3], v[92:95], v[96:99], %[c3]\ns_nop 7\n"
#define CONV_ASM1 \
  "ds_read_b64 v[0:1], %[aA] offset:32\nds_read_b64 v[2:3], %[aA] offset:0\nds_read_b128 v[56:59], %[aB] offset:3200\nds_read_b128 v[52:55], %[aB] offset:2688\n" \
  "ds_read_b128 v[40:43], %[aB] offset:1152\nds_read_b128 v[36:39], %[aB] offset:640\nds_read_b64 v[4:5], %[aA] offset:2080\nds_read_b64 v[6:7], %[aA] offset:2048\n" \
  "ds_read_b128 v[48:51], %[aB] offset:2176\nds_read_b64 v[8:9], %[aA] offset:4128\nds_read_b64 v[10:11], %[aA] offset:4096\nds_read_b128 v[44:47], %[aB] offset:1664\n" \
  "ds_read_b64 v[12:13], %[aA] offset:6176\nds_read_b64 v[14:15], %[aA] offset:6144\nds_read_b128 v[32:35], %[aB] offset:128\nds_read_b64 v[16:17], %[aA] offset:8224\n" \
  "ds_read_b64 v[18:19], %[aA] offset:8192\nds_read_b64 v[20:21], %[aA] offset:10272\nds_read_b64 v[22:23], %[aA] offset:10240\nds_read_b64 v[24:25], %[aA] offset:12320\n" \
  "ds_read_b64 v[26:27], %[aA] offset:12288\ns_waitcnt lgkmcnt(0)\n1:\nv_mfma_f32_16x16x32_f16 %[c3], v[0:3], v[56:59], %[c3]\nds_read_b64 v[64:65], %[aA] offset:288\n" \
  "ds_read_b64 v[66:67], %[aA] offset:256\nv_mfma_f32_16x16x32_f16 %[c2], v[0:3], v[52:55], %[c2]\nds_read_b128 v[120:123], %[aB] offset:3136\n" \
  "ds_read_b128 v[116:119], %[aB] offset:2624\nv_mfma_f32_16x16x32_f16 %[c1], v[0:3], v[40:43], %[c1]\nds_read_b128 v[104:107], %[aB] offset:1088\n" \
  "ds_read_b128 v[100:103], %[aB] offset:576\nv_mfma_f32_16x16x32_f16 %[c0], v[0:3], v[36:39], %[c0]\nds_read_b64 v[68:69], %[aA] offset:2336\n" \
  "ds_read_b64 v[70:71], %[aA] offset:2304\nv_mfma_f32_16x16x32_f16 %[c3], v[4:7], v[52:55], %[c3]\nds_read_b128 v[112:115], %[aB] offset:2112\n" \
  "ds_read_b64 v[72:73], %[aA] offset:4384\nv_mfma_f32_16x16x32_f16 %[c2], v[4:7], v[48:51], %[c2]\nds_read_b64 v[74:75], %[aA] offset:4352\n" \
  "ds_read_b128 v[108:111], %[aB] offset:1600\nv_mfma_f32_16x16x32_f16 %[c1], v[4:7], v[36:39], %[c1]\nds_read_b64 v[76:77], %[aA] offset:6432\n" \
  "v_mfma_f32_16x16x32_f16 %[c0], v[4:7], v[32:35], %[c0]\nds_read_b64 v[78:79], %[aA] offset:6400\nv_mfma_f32_16x16x32_f16 %[c3], v[8:11], v[48:51], %[c3]\n" \
  "ds_read_b128 v[96:99], %[aB] offset:64\nv_mfma_f32_16x16x32_f16 %[c2], v[8:11], v[44:47], %[c2]\nds_read_b64 v[80:81], %[aA] offset:8480\n" \
  "v_mfma_f32_16x16x32_f16 %[c1], v[8:11], v[32:35], %[c1]\nds_read_b64 v[82:83], %[aA] offset:8448\nv_mfma_f32_16x16x32_f16 %[c3], v[12:15], v[44:47], %[c3]\n" \
  "ds_read_b64 v[84:85], %[aA] offset:10528\nv_mfma_f32_16x16x32_f16 %[c2], v[12:15], v[40:43], %[c2]\nds_read_b64 v[86:87], %[aA] offset:10496\n" \
  "v_mfma_f32_16x16x32_f16 %[c3], v[16:19], v[40:43], %[c3]\nds_read_b64 v[88:89], %[aA] offset:12576\nv_mfma_f32_16x16x32_f16 %[c2], v[16:19], v[36:39], %[c2]\n" \
  "ds_read_b64 v[90:91], %[aA] offset:12544\nv_mfma_f32_16x16x32_f16 %[c3], v[20:23], v[36:39], %[c3]\nv_mfma_f32_16x16x32_f16 %[c2], v[20:23], v[32:35], %[c2]\n" \
  "v_mfma_f32_16x16x32_f16 %[c3], v[24:27], v[32:35], %[c3]\ns_sub_u32 %[n], %[n], 1\ns_cmp_eq_u32 %[n], 0\ns_waitcnt lgkmcnt(0)\ns_cbranch_scc1 2f\n" \
  "v_mfma_f32_16x16x32_f16 %[c3], v[64:67], v[120:123], %[c3]\nds_read_b64 v[0:1], %[aA] offset:544\nds_read_b64 v[2:3], %[aA] offset:512\n" \
  "v_mfma_f32_16x16x32_f16 %[c2], v[64:67], v[116:119], %[c2]\nds_read_b128 v[56:59], %[aB] offset:3072\nds_read_b128 v[52:55], %[aB] offset:2560\n" \
  "v_mfma_f32_16x16x32_f16 %[c1], v[64:67], v[104:107], %[c1]\nds_read_b128 v[40:43], %[aB] offset:1024\nds_read_b128 v[36:39], %[aB] offset:512\n" \
  "v_mfma_f32_16x16x32_f16 %[c0], v[64:67], v[100:103], %[c0]\nds_read_b64 v[4:5], %[aA] offset:2592\nds_read_b64 v[6:7], %[aA] offset:2560\n" \
  "v_mfma_f32_16x16x32_f16 %[c3], v[68:71], v[116:119], %[c3]\nds_read_b128 v[48:51], %[aB] offset:2048\nds_read_b64 v[8:9], %[aA] offset:4640\n" \
  "v_mfma_f32_16x16x32_f16 %[c2], v[68:71], v[112:115], %[c2]\nds_read_b64 v[10:11], %[aA] offset:4608\nds_read_b128 v[44:47], %[aB] offset:1536\n" \
  "v_mfma_f32_16x16x32_f16 %[c1], v[68:71], v[100:103], %[c1]\nds_read_b64 v[12:13], %[aA] offset:6688\nv_mfma_f32_16x16x32_f16 %[c0], v[68:71], v[96:99], %[c0]\n" \
  "ds_read_b64 v[14:15], %[aA] offset:6656\nv_mfma_f32_16x16x32_f16 %[c3], v[72:75], v[112:115], %[c3]\nds_read_b128 v[32:35], %[aB] offset:0\n" \
  "v_mfma_f32_16x16x32_f16 %[c2], v[72:75], v[108:111], %[c2]\nds_read_b64 v[16:17], %[aA] offset:8736\nv_mfma_f32_16x16x32_f16 %[c1], v[72:75], v[96:99], %[c1]\n" \
  "ds_read_b64 v[18:19], %[aA] offset:8704\nv_mfma_f32_16x16x32_f16 %[c3], v[76:79], v[108:111], %[c3]\nds_read_b64 v[20:21], %[aA] offset:10784\n" \
  "v_mfma_f32_16x16x32_f16 %[c2], v[76:79], v[104:107], %[c2]\nds_read_b64 v[22:23], %[aA] offset:10752\nv_mfma_f32_16x16x32_f16 %[c3], v[80:83], v[104:107], %[c3]\n" \
  "ds_read_b64 v[24:25], %[aA] offset:12832\nv_mfma_f32_16x16x32_f16 %[c2], v[80:83], v[100:103], %[c2]\nds_read_b64 v[26:27], %[aA] offset:12800\n" \
  "v_mfma_f32_16x16x32_f16 %[c3], v[84:87], v[100:103], %[c3]\nv_mfma_f32_16x16x32_f16 %[c2], v[84:87], v[96:99], %[c2]\n" \
  "v_mfma_f32_16x16x32_f16 %[c3], v[88:91], v[96:99], %[c3]\nv_add_u32 %[aA], 0x200, %[aA]\nv_subrev_u32 %[aB], 0x80, %[aB]\ns_cmp_lg_u32 %[wv], 0\n" \
  "s_waitcnt vmcnt(0) lgkmcnt(0)\ns_barrier\ns_cbranch_scc1 1b\ns_mov_b64 exec, 1\nbuffer_store_dwordx2 %[tag], off, %[ws], %[fo] sc1\ns_mov_b64 exec, -1\ns_branch 1b\n2:\n" \
  "v_mfma_f32_16x16x32_f16 %[c3], v[64:67], v[120:123], %[c3]\nv_mfma_f32_16x16x32_f16 %[c2], v[64:67], v[116:119], %[c2]\n" \
  "v_mfma_f32_16x16x32_f16 %[c1], v[64:67], v[104:107], %[c1]\nv_mfma_f32_16x16x32_f16 %[c0], v[64:67], v[100:103], %[c0]\n" \
  "v_mfma_f32_16x16x32_f16 %[c3], v[68:71], v[116:119], %[c3]\nv_mfma_f32_16x16x32_f16 %[c2], v[68:71], v[112:115], %[c2]\n" \
  "v_mfma_f32_16x16x32_f16 %[c1], v[68:71], v[100:103], %[c1]\nv_mfma_f32_16x16x32_f16 %[c0], v[68:71], v[96:99], %[c0]\n" \
  "v_mfma_f32_16x16x32_f16 %[c3], v[72:75], v[112:115], %[c3]\nv_mfma_f32_16x16x32_f16 %[c2], v[72:75], v[108:111], %[c2]\n" \
  "v_mfma_f32_16x16x32_f16 %[c1], v[72:75], v[96:99], %[c1]\nv_mfma_f32_16x16x32_f16 %[c3], v[76:79], v[108:111], %[c3]\n" \
  "v_mfma_f32_16x16x32_f16 %[c2], v[76:79], v[104:107], %[c2]\nv_mfma_f32_16x16x32_f16 %[c3], v[80:83], v[104:107], %[c3]\n" \
  "v_mfma_f32_16x16x32_f16 %[c2], v[80:83], v[100:103], %[c2]\nv_mfma_f32_16x16x32_f16 %[c3], v[84:87], v[100:103], %[c3]\n" \
  "v_mfma_f32_16x16x32_f16 %[c2], v[84:87], v[96:99], %[c2]\nv_mfma_f32_16x16x32_f16 %[c3], v[88:91], v[96:99], %[c3]\ns_nop 7\n"
#define CONV_CLOB "v0", "v1", "v2", "v3", "v4", "v5", "v6", "v7", "v8", "v9", "v10", "v11", "v12", "v13", "v14", "v15", "v16", "v17", "v18", "v19", "v20", "v21", "v22", "v23", "v24", "v25", "v26", "v27", "v28", "v29", "v30", "v31", "v32", "v33", "v34", "v35", "v36", "v37", "v38", "v39", "v40", "v41", "v42", "v43", "v44", "v45", "v46", "v47", "v48", "v49", "v50", "v51", "v52", "v53", "v54", "v55", "v56", "v57", "v58", "v59", "v60", "v61", "v62", "v63", "v64", "v65", "v66", "v67", "v68", "v69", "v70", "v71", "v72", "v73", "v74", "v75", "v76", "v77", "v78", "v79", "v80", "v81", "v82", "v83", "v84", "v85", "v86", "v87", "v88", "v89", "v90", "v91", "v92", "v93", "v94", "v95", "v96", "v97", "v98", "v99", "v100", "v101", "v102", "v103", "v104", "v105", "v106", "v107", "v108", "v109", "v110", "v111", "v112", "v113", "v114", "v115", "v116", "v117", "v118", "v119", "v120", "v121", "v122", "v123", "v124", "v125", "v126", "v127"
DI void conv_mma(rsrc_t ws, u32x2 tag, unsigned fo, int wave, int c, int ts, int r0, int n, int lane, f32x4 (&acc)[4]) {
  const int b = lane & 15, g = lane >> 4;
#pragma unroll
  for (int t = 0; t < 4; t++) acc[t] = f32x4{0.f, 0.f, 0.f, 0.f};
  unsigned aa = L_W4 + c * W4_BYTES + (28 + 32 * r0 + b - 8 * g) * 8, ba = L_U + c * U_BYTES + (240 - 32 * r0 + 16 * b + 8 * g) * 2 - 128;
#define CONV_OPS : [c0] "+v"(acc[0]), [c1] "+v"(acc[1]), [c2] "+v"(acc[2]), [c3] "+v"(acc[3]), [aA] "+v"(aa), [aB] "+v"(ba), [n] "+s"(n) \
                 : [wv] "s"(wave), [tag] "v"(tag), [ws] "s"(ws), [fo] "s"(fo) : "memory", "scc", CONV_CLOB
  if (ts) asm volatile(CONV_ASM1 CONV_OPS); else asm volatile(CONV_ASM0 CONV_OPS);
}
DI void conv_store(rsrc_t ws, int ch, int c, int ts, int j, int lane, f32x4 y) {
  const int sh = ldsr<int>(L_MISC + 32 + c * 4) - 112 - ldsr<int>(L_MISC + 16 + c * 4);
  const int tau = 2 * j + ((j & 1) ^ ts);
  st8(ws, WS_YT + ch * 4096 + (256 * tau + 16 * (lane & 15) + 4 * (lane >> 4)) * 2,
      u32x2{pk_bf16(ldexpf(y[0], sh), ldexpf(y[1], sh)), pk_bf16(ldexpf(y[2], sh), ldexpf(y[3], sh))});
}

#define E1(b, i) "v_exp_f32 v[" S(b) "+" S(i) "], v[" S(b) "+" S(i) "]\n"
#define E2(b, i) E1(b, i) E1(b, i + 1)
#define E4(b, i) E2(b, i) E2(b, i + 2)
#define A_SM(O, V, s, lo) "v_smfmac_f32_32x32x32_bf16 %[" O "], v[" S(V) ":" S(V) "+3], v[" S(s) "+" S(lo) ":" S(s) "+" S(lo) "+7], %[idx]\n"
#define A_MA(far, Q, N) "v_mfma_f32_32x32x16_bf16 v[" S(far) ":" S(far) "+15], v[72:75], %[" Q "], %[" N "]\n"
#define A_MB(far, KR, Q) "v_mfma_f32_32x32x8_bf16 v[" S(far) ":" S(far) "+15], v[" S(KR) ":" S(KR) "+1], %[" Q "], v[" S(far) ":" S(far) "+15]\n"
#define A_VO(T) "11264*((" S(T) ")/2)+2560+1536*((" S(T) ")&1)"
#define A_RV(V, T, o) "ds_read_b64_tr_b16 v[" S(V) ":" S(V) "+1], %[aV] offset:" A_VO(T) "+" S(o) "\n ds_read_b64_tr_b16 v[" S(V) "+2:" S(V) "+3], %[aV2] offset:" A_VO(T) "+" S(o) "\n"
#define A_RK(T) "ds_read_b128 v[72:75], %[aK] offset:11264*((" S(T) ")/2)+1024*((" S(T) ")&1)\n"
#define A_RR(KR, PR) "ds_read_b64 v[" S(KR) ":" S(KR) "+1], %[aR] offset:11264*(" S(PR) ")\n"
#define A_W(n) "s_waitcnt lgkmcnt(" #n ")\n"
#define A_D(v, o) "global_load_lds_dwordx4 v[" S(v) "], s[40:41] offset:" S(o) " sc1\n"
#define A_X(v, o) "s_cbranch_scc0 9f\n global_load_lds_dwordx4 v[" S(v) "], s[40:41] offset:" S(o) "\n 9:\n"
#define A_XM "s_cbranch_scc0 9f\n s_mov_b32 m0, s42\n 9:\n"
#define A_TILE(T, a, b, fa, fb, KR, QA, QB, XR, WA, WB, DA, DB) \
  E2(a, 0) A_SM("OB", 68, fb, 8) A_RV(68, T, 768) XR E4(a, 2) WA A_MA(fa, "QmA", "NA") E4(a, 6) A_SM("OA", 64, a, 0) DA E4(a, 10) A_MB(fa, KR, QA) E2(a, 14) \
  E2(b, 0) WB A_SM("OA", 68, a, 8) DB E4(b, 2) A_MA(fb, "QmB", "NB") A_RK(T + 2) E4(b, 6) A_SM("OB", 64, b, 0) A_RV(64, T + 1, 0) E4(b, 10) A_MB(fb, KR, QB) E2(b, 14)
#define A_PAIR(T, KRc, KRn, D0, D1, D2, D3) \
  A_TILE(T, 0, 16, 32, 48, KRc, "QA1", "QB1", A_RR(KRn, (T) / 2 + 1), A_W(3), A_W(1), D0, D1) \
  A_TILE(T + 1, 32, 48, 0, 16, KRn, "QA0", "QB0", "", A_W(2), A_W(0), D2, D3)
#define A_BODY \
  A_PAIR(0, 76, 78, A_D(80, -3072), A_D(81, -2048), A_D(82, -1024), A_D(83, 0)) \
  A_PAIR(2, 78, 76, A_D(84, 1024), A_D(85, 2048), A_XM, A_X(86, -3072)) \
  A_PAIR(4, 76, 78, A_X(87, -2048), A_X(88, -1024), A_X(89, 0), A_X(90, 1024)) \
  A_PAIR(6, 78, 76, A_X(91, 2048), A_X(92, 3072), "", "")

DI f32x16 qk(u32x4 Km, u32x2 Kr, u32x4 Qm, u32x2 Qr, f32x16 c) {
  c = __builtin_amdgcn_mfma_f32_32x32x16_bf16(__builtin_bit_cast(bf16x8, Km), __builtin_bit_cast(bf16x8, Qm), c, 0, 0, 0);
  return __builtin_amdgcn_mfma_f32_32x32x8bf16_1k(__builtin_bit_cast(s16x4, Kr), __builtin_bit_cast(s16x4, Qr), c, 0, 0, 0);
}
DI f32x16 splat(float v) { f32x16 r;
#pragma unroll
  for (int i = 0; i < 16; i++) r[i] = v;
  return r; }
DI float max16(f32x16 s) { float m = s[0];
#pragma unroll
  for (int i = 1; i < 16; i++) m = fmaxf(m, s[i]);
  return m; }
DI float other_half(float v) {
  const auto r = __builtin_amdgcn_permlane32_swap(__float_as_uint(v), __float_as_uint(v), false, false);
  return __uint_as_float((threadIdx.x & 32) ? r[0] : r[1]);
}
DI f32x16 attn_exact(rsrc_t ws, unsigned kvb, u32x4 Qm, u32x2 Qr0, int lane) {
  const int h = lane >> 5, r = lane & 31;
  float m = -INFINITY;
  f32x16 O = splat(0.f);
#pragma unroll 1
  for (int ps = 0; ps < 2; ps++) {
#pragma unroll 1
    for (int t = 0; t < 64; t++) {
      const unsigned pb = kvb + (t >> 1) * PAIR;
      const f32x16 s = qk(ld16(ws, pb + (t & 1) * 1024 + lane * 16), ld8(ws, pb + 2048 + (t & 1) * 256 + r * 8), Qm, Qr0, splat(ps ? -m : 0.f));
      if (!ps) { m = fmaxf(m, max16(s)); continue; }
#pragma unroll 1
      for (int hf = 0; hf < 2; hf++) {
        f32x8 pe; u32x4 V;
#pragma unroll
        for (int i = 0; i < 8; i++) pe[i] = __builtin_amdgcn_exp2f(hf ? s[8 + i] : s[i]);
#pragma unroll
        for (int i = 0; i < 8; i++) {
          const unsigned key = 8 * h + i;
          const unsigned o = pb + 2560 + (t & 1) * 1536 + hf * 768 + (r < 16 ? key * 32 + r * 2 : 512 + vtail_pos(key >> 2) * 64 + (key & 3) * 16 + min(r - 16, 7) * 2);
          const unsigned x = __builtin_amdgcn_raw_buffer_load_b16(ws, o, 0, SC1);
          if (i & 1) V[i >> 1] |= x << 16; else V[i >> 1] = x;
        }
        O = __builtin_amdgcn_smfmac_f32_32x32x32_bf16(__builtin_bit_cast(bf16x8, V), __builtin_bit_cast(bf16x16, pe), O, 0xdddd, 0, 0);
      }
    }
    m = fmaxf(m, other_half(m));
  }
  return O;
}

__global__ void __launch_bounds__(512, 2) fused(P p) {
  const int b = blockIdx.x, xcd = b & 7, bi = b >> 3, tid = threadIdx.x;
  const int wave = __builtin_amdgcn_readfirstlane(tid >> 6), lane = tid & 63;
  const rsrc_t ws = __builtin_amdgcn_make_buffer_rsrc(p.ws, 0, WS_END, 0x00020000);
  const int rj = xcd * 4 + (bi >> 3), cj = bi & 7;
  const int nch = b < 128 ? 3 : 2, ch0 = b < 128 ? 3 * b : 2 * b + 128;
  STAMP(0);
  const int rb = (xcd >> 1) * 4 + (bi >> 3), cg = (xcd & 1) * 8 + (bi & 7);
  if (wave < 4) {
    gemm_phase(p, ws, rb, cg, rj, cj, wave, lane);
    STAMP(3);
  } else {
    const int c = wave - 4;
    if (wave == 7) loader(p, ws, rb, cg, rj, cj, ch0, nch, lane);
    else if (c < nch) kgen(p, ws, ch0 + c, c, F_U + (cj * 32 + rj) * 8, lane);
    else {
#pragma unroll 1
      for (int s = 0; s < 12; s++) { bar(); STAMP(32 + s); }
    }
    STAMP(5);
    u32x4 v[4];
    if (c < nch) {
#pragma unroll
      for (int i = 0; i < 4; i++) v[i] = ld16(ws, WS_UT + (ch0 + c) * 4096 + i * 1024 + lane * 16);
    }
    windows(tid - 256, nch);
    STAMP(6);
    if (c < nch) stage_u(c, lane, v);
    STAMP(7);
  }
  bar();
  const u32x2 tag = call_tag();
  STAMP(8);
  const int head = rj, chunk = cj;
  const char* kvg = p.ws + WS_KV + head * 16 * POS;
  const int qg = wave & 3, kh = wave >> 2, h = lane >> 5, q = lane & 31;
  f32x4 acc[4], y0, y1, y2;
  const int ts = wave & 1;
  const unsigned ra = L_RED + lane * 16;
#pragma unroll 1
  for (int rd = 0; rd < nch - 1; rd++) {
    const int rq = rd ? wave >> 1 : (wave >> 1) & 1;
    conv_mma(ws, tag, F_Q + (cg * 16 + rb) * 8, wave, rd ? 2 : wave >> 2, ts, rd ? 2 * rq : 4 * rq, 2 - rd, lane, acc);
    if (rd == 0) {
      ldsw<f32x4>(ra + wave * 2048, rq ? acc[0] : acc[2]); ldsw<f32x4>(ra + wave * 2048 + 1024, rq ? acc[1] : acc[3]);
      y0 = rq ? acc[2] : acc[0]; y1 = rq ? acc[3] : acc[1];
      STAMP(9);
    } else {
#pragma unroll
      for (int j = 1; j < 4; j++) { const int o = (rq + j) & 3; ldsw<f32x4>(ra + 16384 + ((o * 2 + ts) * 3 + j - 1) * 1024, o == 0 ? acc[0] : o == 1 ? acc[1] : o == 2 ? acc[2] : acc[3]); }
      y2 = rq == 0 ? acc[0] : rq == 1 ? acc[1] : rq == 2 ? acc[2] : acc[3];
    }
  }
  STAMP(26);
  if (wave == 0) tags_wait(ws, F_Q + (lane < 16 ? (32 + head) / 6 * 16 + lane : lane < 32 ? (64 + head) / 6 * 16 + lane - 16 : head / 6 * 16 + 2 * chunk + (lane & 1)) * 8, tag);
  bar();
  STAMP(10);
  u32x4 QmA, QmB; u32x2 qa, qb;
  const unsigned qo = WS_Q + (head * 64 + chunk * 8 + qg * 2) * QT;
  asm volatile("buffer_load_dwordx4 %0, %4, %6, 0 offen sc1\n buffer_load_dwordx4 %1, %4, %6, 0 offen offset:1280 sc1\n"
               "buffer_load_dwordx2 %2, %5, %6, 0 offen sc1\n buffer_load_dwordx2 %3, %5, %6, 0 offen offset:1280 sc1"
               : "={v[128:131]}"(QmA), "={v[132:135]}"(QmB), "={v[136:137]}"(qa), "={v[138:139]}"(qb) : "v"(qo + lane * 16), "v"(qo + 1024 + q * 8), "s"(ws));
  {
    set_m0(wave * 7168 + 3072);
    const char* s0 = kvg + wave * 7168 + 3072; const unsigned vo = lane * 16;
    dma<-3072, true>(s0, vo); dma<-2048, true>(s0, vo); dma<-1024, true>(s0, vo); dma<0, true>(s0, vo); dma<1024, true>(s0, vo); dma<2048, true>(s0, vo); dma<3072, true>(s0, vo);
  }
  { const int c = wave >> 2, rq = (wave >> 1) & 1;
    conv_store(ws, ch0 + c, c, ts, 2 * rq, lane, y0 + ldsr<f32x4>(ra + (wave ^ 2) * 2048));
    conv_store(ws, ch0 + c, c, ts, 2 * rq + 1, lane, y1 + ldsr<f32x4>(ra + (wave ^ 2) * 2048 + 1024)); }
  if (nch == 3) {
#pragma unroll
    for (int j = 0; j < 3; j++) y2 += ldsr<f32x4>(ra + 16384 + (wave * 3 + j) * 1024);
    conv_store(ws, ch0 + 2, 2, ts, wave >> 1, lane, y2);
  }
  STAMP(11);
  unsigned aK = kh * PAIR + lane * 16, aR = kh * PAIR + 2048 + lane * 8, aV, aV2;
  { const int blk = (lane >> 4) & 1, r = (lane >> 2) & 3, c = lane & 3;
    aV = kh * PAIR + (blk ? 512 + vtail_pos(2 * h) * 64 + r * 16 + (c ? 8 : 0) : (8 * h + r) * 32 + c * 8);
    aV2 = kh * PAIR + (blk ? 512 + vtail_pos(2 * h + 1) * 64 + r * 16 + (c ? 8 : 0) : (8 * h + 4 + r) * 32 + c * 8); }
  u32x16 vd; u32x8 vw;
  { int row = (wave * 13 * 64 + lane) / 10, col = (wave * 13 * 64 + lane) % 10;
#pragma unroll
    for (int k = 0; k < 13; k++) {
      const unsigned o = min(row, 639) * 1280 + col * 16 + 7168 - (k < 6 ? k : k - 6) * 1024;
      if (k < 6) { vw[k] = o; vd[k] = lane * 16; } else vd[k] = o;
      row += 6; col += 4; if (col >= 10) { col -= 10; row++; }
    }
    vd[13] = vd[14] = vd[15] = 0; vw[6] = vw[7] = 0; }
  f32x16 NA, NB, OA = splat(0.f), OB = splat(0.f), S0, S1, S2, S3;
  u32x4 V0, V1 = {0, 0, 0, 0}, Km; u32x2 KrE, KrO, fy;
#pragma unroll
  for (int i = 8; i < 16; i++) S3[i] = 0.f;
  asm volatile("" : "+v"(OA), "+v"(OB), "+{v[48:63]}"(S3), "+{v[68:71]}"(V1), "+{v[80:95]}"(vd), "+{v[98:105]}"(vw));
  asm volatile("s_cmp_eq_u32 %4, 3\n s_cbranch_scc1 1f\n s_waitcnt vmcnt(2)\n 1:\n s_waitcnt vmcnt(3)"
               : "+{v[128:131]}"(QmA), "+{v[132:135]}"(QmB), "+{v[136:137]}"(qa), "+{v[138:139]}"(qb) : "s"(nch) : "memory", "scc");
  bar();
  STAMP(12);
  const u32x2 z2 = {0, 0};
  const u32x2 QA0 = h ? z2 : qa, QA1 = h ? qa : z2, QB0 = h ? z2 : qb, QB1 = h ? qb : z2;
  {
    const u32x4 k0 = ldsr<u32x4>(lane * 16); const u32x2 r0 = ldsr<u32x2>(2048 + lane * 8);
    Km = ldsr<u32x4>(aK); KrE = ldsr<u32x2>(aR);
    float ma = max16(qk(k0, r0, QmA, QA0, splat(0.f))), mb = max16(qk(k0, r0, QmB, QB0, splat(0.f)));
    NA = splat(-fmaxf(ma, other_half(ma))); NB = splat(-fmaxf(mb, other_half(mb)));
    S0 = qk(Km, KrE, QmA, QA0, NA); S1 = qk(Km, KrE, QmB, QB0, NB);
    Km = ldsr<u32x4>(aK + 1024);
    V0 = cat(ldtr(aV + 2560), ldtr(aV2 + 2560));
  }
  const unsigned idx = 0xddddu;
  const unsigned o0 = (80 * cj < 384) ? 80 * cj / 3 : 40 * cj - 64, o1 = (80 * cj + 79 < 384) ? (80 * cj + 79) / 3 : (80 * cj + 79) / 2 - 64;
  const unsigned fyo = F_Y + min(o0 + lane, o1) * 8;
  STAMP(13);
#pragma unroll 1
  for (int it = 0; it < 4; it++) {
    const char* src = kvg + (it == 2 ? 12 : 5 + 4 * it) * POS + wave * 6144 + 3072;
    unsigned dst = (it == 2 ? 0 : 5 + 4 * it) * POS + wave * 6144 + 3072;
    if (it) {
      WAITV(0); bar();
      if (it == 1) { if (tid == 0) st8(ws, F_Y + b * 8, tag); }
    }
    if (it == 3) { src = p.wo + cj * 160 - 4096; dst = L_WO + wave * 13312 + 3072; }
    asm volatile("s_mov_b64 s[40:41], %[src]\n s_mov_b32 m0, %[dst]\n s_add_u32 s42, %[dst], 6144\n s_cmp_eq_u32 %[it], 3\n s_nop 6\n"
                 "s_cbranch_scc0 9f\n buffer_load_dwordx2 v[96:97], %[fyo], %[ws], 0 offen sc1\n"
                 "v_mov_b64 v[80:81], v[98:99]\n v_mov_b64 v[82:83], v[100:101]\n v_mov_b64 v[84:85], v[102:103]\n 9:\n"
                 A_BODY "s_waitcnt vmcnt(13) lgkmcnt(0)\n"
                 : [OA] "+v"(OA), [OB] "+v"(OB), "+{v[0:15]}"(S0), "+{v[16:31]}"(S1), "+{v[32:47]}"(S2), "+{v[48:63]}"(S3),
                   "+{v[64:67]}"(V0), "+{v[68:71]}"(V1), "+{v[72:75]}"(Km), "+{v[76:77]}"(KrE), "+{v[78:79]}"(KrO), "=&{v[96:97]}"(fy), "+{v[80:95]}"(vd)
                 : [NA] "v"(NA), [NB] "v"(NB), [QmA] "{v[128:131]}"(QmA), [QmB] "{v[132:135]}"(QmB), [QA0] "v"(QA0), [QA1] "v"(QA1), [QB0] "v"(QB0), [QB1] "v"(QB1),
                   [idx] "v"(idx), [aK] "v"(aK), [aR] "v"(aR), [aV] "v"(aV), [aV2] "v"(aV2), "{v[98:105]}"(vw), [src] "s"(src), [dst] "s"(dst), [it] "s"(it), [fyo] "v"(fyo), [ws] "s"(ws)
                 : "memory", "scc", "s40", "s41", "s42");
    const unsigned d = it == 2 ? (unsigned)(-8 * (int)POS) : 4 * POS;
    aK += d; aR += d; aV += d; aV2 += d;
  }
  asm volatile("s_nop 1\n v_smfmac_f32_32x32x32_bf16 %[O], v[68:71], v[56:63], %[idx]\n s_nop 15\n"
               : [O] "+v"(OB) : "{v[48:63]}"(S3), "{v[68:71]}"(V1), [idx] "v"(idx));
  STAMP(14);
  if (__builtin_amdgcn_ballot_w64(fy[0] != tag[0] || fy[1] != tag[1]) != 0) tags_wait(ws, fyo, tag);
  asm volatile("" ::: "memory");
  u32x4 yv[2], uu[2], dd[2];
#pragma unroll
  for (int i = 0; i < 2; i++) {
    const int c = min(tid + 512 * i, 639), gt = min(wave + 8 * i, 9) * 4 + (lane >> 4);
    yv[i] = ld16(ws, WS_YT + (cj * 80 + (c >> 3)) * 4096 + rj * 128 + (c & 7) * 16);
    uu[i] = *(const GAS u32x4*)(p.u + (rj * 64 + gt / 10 * 16 + (lane & 15)) * 1280 + cj * 160 + gt % 10 * 16);
    dd[i] = *(const GAS u32x4*)(p.dsk + cj * 160 + gt % 10 * 16);
  }
  bar();
  {
    const f32x16 snd = kh ? OA : OB;
#pragma unroll
    for (int i = 0; i < 3; i++) ldsw<f32x4>(wave * 3072 + i * 1024 + lane * 16, f32x4{snd[4 * i], snd[4 * i + 1], snd[4 * i + 2], snd[4 * i + 3]});
  }
  bar();
  f32x16 O = kh ? OB : OA;
#pragma unroll
  for (int i = 0; i < 3; i++) { const f32x4 t = ldsr<f32x4>((wave ^ 4) * 3072 + i * 1024 + lane * 16);
#pragma unroll
    for (int j = 0; j < 4; j++) O[4 * i + j] += t[j]; }
  {
    float l = O[8], big = 0.f;
    { const float t = other_half(l); l = h ? l : t; }
#pragma unroll
    for (int i = 0; i < 12; i++) big += fabsf(O[i]);
    const bool ok = l >= 0x1p-64f && l <= 0x1p64f && big < INFINITY;
    if (__builtin_amdgcn_ballot_w64(!ok) != 0) {
      O = attn_exact(ws, WS_KV + head * 16 * POS, kh ? QmB : QmA, kh ? QB0 : QA0, lane);
      l = O[8]; const float t = other_half(l); l = h ? l : t;
    }
    const float inv = 1.f / l;
    const unsigned oo = WS_O + (head * 64 + chunk * 8 + qg * 2 + kh) * QT;
    u32x4 v;
#pragma unroll
    for (int i = 0; i < 4; i++) v[i] = pk_bf16(O[2 * i] * inv, O[2 * i + 1] * inv);
    st16(ws, oo + lane * 16, v);
    if (!h) st8(ws, oo + 1024 + q * 8, u32x2{pk_bf16(O[8] * inv, O[9] * inv), pk_bf16(O[10] * inv, O[11] * inv)});
  }
  constexpr unsigned L_Y = 24576;
#pragma unroll
  for (int i = 0; i < 2; i++) { const int c = min(tid + 512 * i, 639); ldsw<u32x4>(L_Y + (c >> 3) * 144 + (c & 7) * 16, yv[i]); }
  WAITV(0); bar();
  if (tid == 0) st8(ws, F_O + (chunk * 32 + head) * 8, tag);
  STAMP(15);
#pragma unroll
  for (int i = 0; i < 2; i++) {
    const int task = wave + 8 * i;
    if (task < 10) {
      const int gt = task * 4 + (lane >> 4), rbk = gt / 10, oct = gt % 10;
      const unsigned a = L_Y + (oct * 8 + ((lane >> 2) & 3)) * 144 + rbk * 32 + (lane & 3) * 8;
      const u32x4 y = cat(ldtr(a), ldtr(a + 4 * 144));
      u32x4 o;
#pragma unroll
      for (int j = 0; j < 4; j++) {
        const float lo = __uint_as_float(y[j] << 16) + rbf(__uint_as_float(uu[i][j] << 16) * __uint_as_float(dd[i][j] << 16));
        const float hi = __uint_as_float(y[j] & 0xffff0000u) + rbf(__uint_as_float(uu[i][j] & 0xffff0000u) * __uint_as_float(dd[i][j] & 0xffff0000u));
        o[j] = pk_bf16(lo, hi);
      }
      *(GAS u32x4*)(p.out + (rj * 64 + rbk * 16 + (lane & 15)) * 2560 + cj * 160 + oct * 16) = o;
    }
  }
  STAMP(16);
  {
    const int wm = wave & 3, wk = wave >> 2, g = lane >> 4, tk = lane & 15;
    const int qq = (lane >> 2) & 3, pp = lane & 3;
    const unsigned am = L_WO + ((g >> 1) * 20 + (g & 1) * 4 + qq) * 160 + pp * 8 + wk * 400 * 160, ar = L_WO + (g * 20 + 16 + qq) * 160 + pp * 8;
    unsigned a0[10], a1[10];
#pragma unroll
    for (int s = 0; s < 10; s++) {
      a0[s] = s < 6 ? am + s * 6400 : wk ? ar + (s - 6) * 25600 : am + s * 6400;
      a1[s] = a0[s] + (s >= 6 && wk ? 12800 : 1280);
    }
    u32x4 A[8][5];
#pragma unroll
    for (int s = 0; s < 8; s++) {
#pragma unroll
      for (int c = 0; c < 5; c++) A[s][c] = cat(ldtr(a0[s] + c * 32), ldtr(a1[s] + c * 32));
      asm volatile("" : "+v"(A[s][0]), "+v"(A[s][1]), "+v"(A[s][2]), "+v"(A[s][3]), "+v"(A[s][4]));
    }
    STAMP(4);
    tags_wait(ws, F_O + ((rj >> 2) * 32 + (lane & 31)) * 8, tag);
    STAMP(17);
    const unsigned ob = WS_O + (rj * 2 + (wm >> 1)) * QT, tok = (wm & 1) * 16 + tk;
    const unsigned mo = ob + (g >> 1) * 64 * QT + (g & 1) * 512 + tok * 16 + wk * 1280 * QT, ro = ob + g * 64 * QT + 1024 + tok * 8;
    u32x4 B[10];
#pragma unroll
    for (int s = 0; s < 6; s++) B[s] = ld16(ws, mo + s * 128 * QT);
    if (wk == 0) {
#pragma unroll
      for (int s = 6; s < 10; s++) B[s] = ld16(ws, mo + s * 128 * QT);
    } else {
#pragma unroll
      for (int s = 0; s < 4; s++) B[6 + s] = cat(ld8(ws, ro + s * 512 * QT), ld8(ws, ro + s * 512 * QT + 256 * QT));
    }
    f32x4 acc[5];
#pragma unroll
    for (int c = 0; c < 5; c++) acc[c] = f32x4{0.f, 0.f, 0.f, 0.f};
#pragma unroll
    for (int s = 0; s < 10; s++) {
#pragma unroll
      for (int c = 0; c < 5; c++) acc[c] = __builtin_amdgcn_mfma_f32_16x16x32_bf16(__builtin_bit_cast(bf16x8, A[s & 7][c]), __builtin_bit_cast(bf16x8, B[s]), acc[c], 0, 0, 0);
      if (s < 2) {
#pragma unroll
        for (int c = 0; c < 5; c++) A[s][c] = cat(ldtr(a0[8 + s] + c * 32), ldtr(a1[8 + s] + c * 32));
      }
    }
    STAMP(18);
    const unsigned ra = wm * 5120 + lane * 16;
#pragma unroll
    for (int c = 0; c < 5; c++) if ((c < 3) == (wk == 1)) ldsw<f32x4>(ra + c * 1024, acc[c]);
    bar();
    char* o = p.out + (rj * 64 + wm * 16 + tk) * 2560 + 1280 + cj * 160 + g * 8;
#pragma unroll
    for (int c = 0; c < 5; c++)
      if ((c < 3) == (wk == 0)) {
        const f32x4 t = acc[c] + ldsr<f32x4>(ra + c * 1024);
        *(GAS u32x2*)(o + c * 32) = u32x2{pk_bf16(t[0], t[1]), pk_bf16(t[2], t[3])};
      }
  }
  STAMP(19);
  if (b == 0 && wave == 0) {
#pragma unroll 1
    for (int i = 0; i < 4; i++) tags_wait(ws, F_U + (i * 64 + lane) * 8, tag);
    if (lane == 0) st8(ws, F_E, tag);
  }
}

extern "C" void kernel_launch(void* const* d_in, const int* in_sizes, int n_in, void* d_out, int out_size, void* d_ws, size_t ws_size,
                              hipStream_t stream) {
  P p;
  p.u = (const char*)d_in[0]; p.lar = (const char*)d_in[1]; p.aim = (const char*)d_in[2]; p.cre = (const char*)d_in[3];
  p.cim = (const char*)d_in[4]; p.ldt = (const char*)d_in[5]; p.dsk = (const char*)d_in[6]; p.x = (const char*)d_in[7];
  p.wqkv = (const char*)d_in[8]; p.wo = (const char*)d_in[9]; p.out = (char*)d_out; p.ws = (char*)d_ws;
  (void)hipFuncSetAttribute((const void*)fused, hipFuncAttributeMaxDynamicSharedMemorySize, LDS_BYTES);
  fused<<<256, 512, LDS_BYTES, stream>>>(p);
}
```

```cpp
#include <hip/hip_runtime.h>
#include <hip/hip_bf16.h>
#include <stdint.h>

typedef float f32x16 __attribute__((ext_vector_type(16)));
typedef float f32x8 __attribute__((ext_vector_type(8)));
typedef float f32x4 __attribute__((ext_vector_type(4)));
typedef float f32x2 __attribute__((ext_vector_type(2)));
typedef unsigned u32x4 __attribute__((ext_vector_type(4)));
typedef unsigned u32x2 __attribute__((ext_vector_type(2)));
typedef __bf16 bf16x8 __attribute__((ext_vector_type(8)));
typedef __bf16 bf16x2 __attribute__((ext_vector_type(2)));
typedef __bf16 bf16x16 __attribute__((ext_vector_type(16)));
typedef short s16x4 __attribute__((ext_vector_type(4)));
typedef unsigned short u16x2 __attribute__((ext_vector_type(2)));
typedef _Float16 f16x8 __attribute__((ext_vector_type(8)));
typedef _Float16 f16x2 __attribute__((ext_vector_type(2)));
#define LAS __attribute__((address_space(3)))
#define GAS __attribute__((address_space(1)))
#define DI __device__ __forceinline__
typedef __amdgpu_buffer_rsrc_t rsrc_t;

constexpr unsigned QT = 1280;
constexpr unsigned PAIR = 5632;
constexpr unsigned POS = 2 * PAIR;
constexpr unsigned WS_Q = 0;
constexpr unsigned WS_KV = WS_Q + 32u * 64u * QT;
constexpr unsigned WS_O = WS_KV + 32u * 16u * POS + 4096;
constexpr unsigned WS_OT = WS_O + 32u * 64u * 1024u;
constexpr unsigned WS_UT = WS_O + 32u * 64u * QT;
constexpr unsigned WS_YT = WS_UT + 640u * 4096u;
constexpr unsigned WS_F = WS_YT + 640u * 4096u;
constexpr unsigned F_U = WS_F, F_Q = WS_F + 2048, F_Y = WS_F + 4096, F_O = WS_F + 6144, F_E = WS_F + 8192;
constexpr unsigned WS_END = WS_F + 16384;
constexpr int LDS_BYTES = 160 * 1024;

constexpr unsigned W4_BYTES = 2064 * 8, U_BYTES = (256 + 2048) * 2;
constexpr unsigned L_MISC = 98304;
constexpr unsigned L_W4 = LDS_BYTES - 3 * W4_BYTES - 3 * U_BYTES, L_U = L_W4 + 3 * W4_BYTES;
constexpr unsigned L_PAR = L_U + 10240;
constexpr unsigned L_KC = 65536;
constexpr unsigned L_RED = 5 * POS + 1024;
constexpr unsigned L_WO = 4 * POS;
static_assert(L_MISC + 64 <= L_W4 && L_RED + 32768 <= L_MISC && L_WO + 102400 <= LDS_BYTES, "lds map");

#define OUT_AUX_A 0
#define OUT_AUX_B 0
#define POLL_SLEEP 1
#define KG_PRIO 0
struct P {
  const char *u, *lar, *aim, *cre, *cim, *ldt, *dsk, *x, *wqkv, *wo;
  char *out, *ws;
};

DI float bf2f(unsigned short v) { return __uint_as_float((unsigned)v << 16); }
DI unsigned pk_bf16(float a, float b) { bf16x2 r; r[0] = (__bf16)a; r[1] = (__bf16)b; return __builtin_bit_cast(unsigned, r); }
DI unsigned pk_f16(float a, float b) { f16x2 r; r[0] = (_Float16)a; r[1] = (_Float16)b; return __builtin_bit_cast(unsigned, r); }
DI float rbf(float a) { return (float)(__bf16)a; }
template <class T> DI T ldsr(unsigned a) { return *(const LAS T*)(size_t)a; }
template <class T> DI void ldsw(unsigned a, T v) { *(LAS T*)(size_t)a = v; }
DI u32x2 ldtr(unsigned a) { return __builtin_bit_cast(u32x2, __builtin_amdgcn_ds_read_tr16_b64_v4i16((LAS s16x4*)(size_t)a)); }
DI u32x4 cat(u32x2 a, u32x2 b) { u32x4 r; r[0] = a[0]; r[1] = a[1]; r[2] = b[0]; r[3] = b[1]; return r; }
DI u32x2 call_tag() { const u32x2 e = ldsr<u32x2>(L_MISC); return u32x2{e[0] * 0x9E3779B1u + 0x7F4A7C15u, e[1] * 0x85EBCA77u + 0x165667B1u}; }
DI void bar() { asm volatile("s_waitcnt lgkmcnt(0)\n\ts_barrier" ::: "memory"); }
#define WAITV(n) asm volatile("s_waitcnt vmcnt(" #n ")" ::: "memory")
constexpr int SC1 = 16;
DI u32x4 ld16(rsrc_t r, unsigned o) { return __builtin_amdgcn_raw_buffer_load_b128(r, o, 0, SC1); }
DI u32x2 ld8(rsrc_t r, unsigned o) { return __builtin_amdgcn_raw_buffer_load_b64(r, o, 0, SC1); }
DI void st16(rsrc_t r, unsigned o, u32x4 v) { __builtin_amdgcn_raw_buffer_store_b128(v, r, o, 0, SC1); }
DI void st8(rsrc_t r, unsigned o, u32x2 v) { __builtin_amdgcn_raw_buffer_store_b64(v, r, o, 0, SC1); }
DI u32x4 ld16s(rsrc_t r, unsigned o, unsigned so) { return __builtin_amdgcn_raw_buffer_load_b128(r, o, so, SC1); }
DI u32x2 ld8s(rsrc_t r, unsigned o, unsigned so) { return __builtin_amdgcn_raw_buffer_load_b64(r, o, so, SC1); }
template <int AUX> DI void st16x(rsrc_t r, unsigned o, unsigned so, u32x4 v) { __builtin_amdgcn_raw_buffer_store_b128(v, r, o, so, AUX); asm volatile("s_nop 1" :: "v"(v) : "memory"); }
DI void st16s(rsrc_t r, unsigned o, unsigned so, u32x4 v) { st16x<SC1>(r, o, so, v); }
DI void st8s(rsrc_t r, unsigned o, unsigned so, u32x2 v) { __builtin_amdgcn_raw_buffer_store_b64(v, r, o, so, SC1); }
DI bool tags_ready(rsrc_t r, unsigned o, u32x2 tag) {
  u32x2 v = ld8(r, o);
  asm volatile("" : "+v"(v) :: "memory");
  return __builtin_amdgcn_ballot_w64(v[0] != tag[0] || v[1] != tag[1]) == 0;
}
DI void tags_wait(rsrc_t r, unsigned o, u32x2 tag) {
#pragma unroll 1
  for (int i = 0; i < (1 << 16); i++) { if (tags_ready(r, o, tag)) break; __builtin_amdgcn_s_sleep(POLL_SLEEP); }
}
DI void set_m0(unsigned v) { asm volatile("s_mov_b32 m0, %0\n\ts_nop 0" :: "s"(v)); }
template <int OFF, bool DEV> DI void dma(const char* base, unsigned voff) {
  if (DEV) asm volatile("global_load_lds_dwordx4 %0, %1 offset:%2 sc1" :: "v"(voff), "s"(base), "i"(OFF));
  else asm volatile("global_load_lds_dwordx4 %0, %1 offset:%2" :: "v"(voff), "s"(base), "i"(OFF));
}
#define STAMP(i)
#define STAMPX(i)
#define STAMPY(i)
#define XS(x) #x
#define S(x) XS(x)

DI unsigned v_off(unsigned blk, unsigned key) { return blk * 256 + (((key >> 2) ^ blk) * 64) + (key & 3) * 16; }
#define GT0 ""
#define GT1 ""
#define GT2 ""
#define GEMM_ASM \
  "s_mov_b32 s42, 1\ns_mov_b32 %[ok], 0\nds_read_b128 v[64:67], %[x0] offset:0\nds_read_b64_tr_b16 v[80:81], %[w0] offset:0\nds_read_b64_tr_b16 v[82:83], %[w0] offset:1024\n" \
  "ds_read_b128 v[68:71], %[x0] offset:2048\nds_read_b64_tr_b16 v[84:85], %[w1] offset:0\nds_read_b64_tr_b16 v[86:87], %[w1] offset:1024\n" \
  "ds_read_b128 v[72:75], %[x0] offset:4096\nds_read_b64_tr_b16 v[88:89], %[w2] offset:0\nds_read_b64_tr_b16 v[90:91], %[w2] offset:1024\n" \
  "ds_read_b128 v[76:79], %[x0] offset:6144\nds_read_b64_tr_b16 v[92:93], %[w3] offset:0\nds_read_b64_tr_b16 v[94:95], %[w3] offset:1024\n" \
  "ds_read_b128 v[96:99], %[x1] offset:0\nds_read_b64_tr_b16 v[112:113], %[w0] offset:8192\nds_read_b64_tr_b16 v[114:115], %[w0] offset:9216\n" \
  "ds_read_b128 v[100:103], %[x1] offset:2048\nds_read_b64_tr_b16 v[116:117], %[w1] offset:8192\nds_read_b64_tr_b16 v[118:119], %[w1] offset:9216\n" \
  "ds_read_b128 v[104:107], %[x1] offset:4096\nds_read_b64_tr_b16 v[120:121], %[w2] offset:8192\nds_read_b64_tr_b16 v[122:123], %[w2] offset:9216\n" \
  "ds_read_b128 v[108:111], %[x1] offset:6144\nds_read_b64_tr_b16 v[124:125], %[w3] offset:8192\nds_read_b64_tr_b16 v[126:127], %[w3] offset:9216\ns_waitcnt lgkmcnt(12)\n" \
  "1:\nv_mfma_f32_16x16x32_bf16 v[0:3], v[80:83], v[64:67], v[0:3]\nv_mfma_f32_16x16x32_bf16 v[4:7], v[84:87], v[64:67], v[4:7]\n" \
  "v_mfma_f32_16x16x32_bf16 v[8:11], v[88:91], v[64:67], v[8:11]\nv_mfma_f32_16x16x32_bf16 v[12:15], v[92:95], v[64:67], v[12:15]\n" \
  "v_mfma_f32_16x16x32_bf16 v[16:19], v[80:83], v[68:71], v[16:19]\nv_mfma_f32_16x16x32_bf16 v[20:23], v[84:87], v[68:71], v[20:23]\n" \
  "v_mfma_f32_16x16x32_bf16 v[24:27], v[88:91], v[68:71], v[24:27]\nv_mfma_f32_16x16x32_bf16 v[28:31], v[92:95], v[68:71], v[28:31]\n" \
  "v_mfma_f32_16x16x32_bf16 v[32:35], v[80:83], v[72:75], v[32:35]\nv_mfma_f32_16x16x32_bf16 v[36:39], v[84:87], v[72:75], v[36:39]\n" \
  "v_mfma_f32_16x16x32_bf16 v[40:43], v[88:91], v[72:75], v[40:43]\nv_mfma_f32_16x16x32_bf16 v[44:47], v[92:95], v[72:75], v[44:47]\n" \
  "v_mfma_f32_16x16x32_bf16 v[48:51], v[80:83], v[76:79], v[48:51]\nv_mfma_f32_16x16x32_bf16 v[52:55], v[84:87], v[76:79], v[52:55]\n" \
  "v_mfma_f32_16x16x32_bf16 v[56:59], v[88:91], v[76:79], v[56:59]\nv_mfma_f32_16x16x32_bf16 v[60:63], v[92:95], v[76:79], v[60:63]\ns_cmp_eq_u32 s42, 10\n" \
  "s_cbranch_scc1 9f\ns_waitcnt lgkmcnt(0)\ns_barrier\n" GT0 "v_mfma_f32_16x16x32_bf16 v[0:3], v[112:115], v[96:99], v[0:3]\nds_read_b128 v[128:131], %[x0] offset:32768\n" \
  "ds_read_b64_tr_b16 v[144:145], %[w0] offset:32768\nv_mfma_f32_16x16x32_bf16 v[4:7], v[116:119], v[96:99], v[4:7]\nds_read_b64_tr_b16 v[146:147], %[w0] offset:33792\n" \
  "ds_read_b128 v[132:135], %[x0] offset:34816\nv_mfma_f32_16x16x32_bf16 v[8:11], v[120:123], v[96:99], v[8:11]\nds_read_b64_tr_b16 v[148:149], %[w1] offset:32768\n" \
  "ds_read_b64_tr_b16 v[150:151], %[w1] offset:33792\nv_mfma_f32_16x16x32_bf16 v[12:15], v[124:127], v[96:99], v[12:15]\nds_read_b128 v[136:139], %[x0] offset:36864\n" \
  "ds_read_b64_tr_b16 v[152:153], %[w2] offset:32768\nv_mfma_f32_16x16x32_bf16 v[16:19], v[112:115], v[100:103], v[16:19]\n" \
  "ds_read_b64_tr_b16 v[154:155], %[w2] offset:33792\nds_read_b128 v[140:143], %[x0] offset:38912\nv_mfma_f32_16x16x32_bf16 v[20:23], v[116:119], v[100:103], v[20:23]\n" \
  "ds_read_b64_tr_b16 v[156:157], %[w3] offset:32768\nds_read_b64_tr_b16 v[158:159], %[w3] offset:33792\n" \
  "v_mfma_f32_16x16x32_bf16 v[24:27], v[120:123], v[100:103], v[24:27]\nds_read_b128 v[64:67], %[x1] offset:32768\nds_read_b64_tr_b16 v[80:81], %[w0] offset:40960\n" \
  "v_mfma_f32_16x16x32_bf16 v[28:31], v[124:127], v[100:103], v[28:31]\nds_read_b64_tr_b16 v[82:83], %[w0] offset:41984\nds_read_b128 v[68:71], %[x1] offset:34816\n" \
  "v_mfma_f32_16x16x32_bf16 v[32:35], v[112:115], v[104:107], v[32:35]\nds_read_b64_tr_b16 v[84:85], %[w1] offset:40960\nds_read_b64_tr_b16 v[86:87], %[w1] offset:41984\n" \
  "v_mfma_f32_16x16x32_bf16 v[36:39], v[116:119], v[104:107], v[36:39]\nds_read_b128 v[72:75], %[x1] offset:36864\nds_read_b64_tr_b16 v[88:89], %[w2] offset:40960\n" \
  "v_mfma_f32_16x16x32_bf16 v[40:43], v[120:123], v[104:107], v[40:43]\nds_read_b64_tr_b16 v[90:91], %[w2] offset:41984\nds_read_b128 v[76:79], %[x1] offset:38912\n" \
  "v_mfma_f32_16x16x32_bf16 v[44:47], v[124:127], v[104:107], v[44:47]\nds_read_b64_tr_b16 v[92:93], %[w3] offset:40960\nds_read_b64_tr_b16 v[94:95], %[w3] offset:41984\n" \
  "v_mfma_f32_16x16x32_bf16 v[48:51], v[112:115], v[108:111], v[48:51]\nv_mfma_f32_16x16x32_bf16 v[52:55], v[116:119], v[108:111], v[52:55]\n" \
  "v_mfma_f32_16x16x32_bf16 v[56:59], v[120:123], v[108:111], v[56:59]\nv_mfma_f32_16x16x32_bf16 v[60:63], v[124:127], v[108:111], v[60:63]\ns_add_u32 s42, s42, 1\n" \
  "s_waitcnt lgkmcnt(12)\nv_mfma_f32_16x16x32_bf16 v[0:3], v[144:147], v[128:131], v[0:3]\nv_mfma_f32_16x16x32_bf16 v[4:7], v[148:151], v[128:131], v[4:7]\n" \
  "v_mfma_f32_16x16x32_bf16 v[8:11], v[152:155], v[128:131], v[8:11]\nv_mfma_f32_16x16x32_bf16 v[12:15], v[156:159], v[128:131], v[12:15]\n" \
  "v_mfma_f32_16x16x32_bf16 v[16:19], v[144:147], v[132:135], v[16:19]\nv_mfma_f32_16x16x32_bf16 v[20:23], v[148:151], v[132:135], v[20:23]\n" \
  "v_mfma_f32_16x16x32_bf16 v[24:27], v[152:155], v[132:135], v[24:27]\nv_mfma_f32_16x16x32_bf16 v[28:31], v[156:159], v[132:135], v[28:31]\n" \
  "v_mfma_f32_16x16x32_bf16 v[32:35], v[144:147], v[136:139], v[32:35]\nv_mfma_f32_16x16x32_bf16 v[36:39], v[148:151], v[136:139], v[36:39]\n" \
  "v_mfma_f32_16x16x32_bf16 v[40:43], v[152:155], v[136:139], v[40:43]\nv_mfma_f32_16x16x32_bf16 v[44:47], v[156:159], v[136:139], v[44:47]\n" \
  "v_mfma_f32_16x16x32_bf16 v[48:51], v[144:147], v[140:143], v[48:51]\nv_mfma_f32_16x16x32_bf16 v[52:55], v[148:151], v[140:143], v[52:55]\n" \
  "v_mfma_f32_16x16x32_bf16 v[56:59], v[152:155], v[140:143], v[56:59]\nv_mfma_f32_16x16x32_bf16 v[60:63], v[156:159], v[140:143], v[60:63]\ns_waitcnt vmcnt(0) lgkmcnt(0)\n" \
  "s_barrier\n" GT1 "s_cmp_eq_u32 s42, %[pub]\ns_cbranch_scc0 2f\ns_mov_b64 exec, 1\nbuffer_store_dwordx2 %[tag], off, %[ws], %[fo] sc1\ns_mov_b64 exec, -1\n2:\n" \
  "s_cmp_eq_u32 s42, 8\ns_cbranch_scc0 4f\nv_cmp_ne_u64 vcc, %[fu], %[tag]\ns_nop 4\ns_cbranch_vccnz 4f\nbuffer_load_dwordx4 v[160:163], %[ua], %[ws], 0 offen offset:0 sc1\n" \
  "buffer_load_dwordx4 v[164:167], %[ua], %[ws], 0 offen offset:1024 sc1\nbuffer_load_dwordx4 v[168:171], %[ua], %[ws], 0 offen offset:2048 sc1\n" \
  "buffer_load_dwordx4 v[172:175], %[ua], %[ws], 0 offen offset:3072 sc1\ns_mov_b32 %[ok], 1\n4:\nv_mfma_f32_16x16x32_bf16 v[0:3], v[80:83], v[64:67], v[0:3]\n" \
  "ds_read_b128 v[96:99], %[x0h] offset:0\nds_read_b64_tr_b16 v[112:113], %[w0h] offset:0\nv_mfma_f32_16x16x32_bf16 v[4:7], v[84:87], v[64:67], v[4:7]\n" \
  "ds_read_b64_tr_b16 v[114:115], %[w0h] offset:1024\nds_read_b128 v[100:103], %[x0h] offset:2048\nv_mfma_f32_16x16x32_bf16 v[8:11], v[88:91], v[64:67], v[8:11]\n" \
  "ds_read_b64_tr_b16 v[116:117], %[w1h] offset:0\nds_read_b64_tr_b16 v[118:119], %[w1h] offset:1024\nv_mfma_f32_16x16x32_bf16 v[12:15], v[92:95], v[64:67], v[12:15]\n" \
  "ds_read_b128 v[104:107], %[x0h] offset:4096\nds_read_b64_tr_b16 v[120:121], %[w2h] offset:0\nv_mfma_f32_16x16x32_bf16 v[16:19], v[80:83], v[68:71], v[16:19]\n" \
  "ds_read_b64_tr_b16 v[122:123], %[w2h] offset:1024\nds_read_b128 v[108:111], %[x0h] offset:6144\nv_mfma_f32_16x16x32_bf16 v[20:23], v[84:87], v[68:71], v[20:23]\n" \
  "ds_read_b64_tr_b16 v[124:125], %[w3h] offset:0\nds_read_b64_tr_b16 v[126:127], %[w3h] offset:1024\nv_mfma_f32_16x16x32_bf16 v[24:27], v[88:91], v[68:71], v[24:27]\n" \
  "ds_read_b128 v[128:131], %[x1h] offset:0\nds_read_b64_tr_b16 v[144:145], %[w0h] offset:8192\nv_mfma_f32_16x16x32_bf16 v[28:31], v[92:95], v[68:71], v[28:31]\n" \
  "ds_read_b64_tr_b16 v[146:147], %[w0h] offset:9216\nds_read_b128 v[132:135], %[x1h] offset:2048\nv_mfma_f32_16x16x32_bf16 v[32:35], v[80:83], v[72:75], v[32:35]\n" \
  "ds_read_b64_tr_b16 v[148:149], %[w1h] offset:8192\nds_read_b64_tr_b16 v[150:151], %[w1h] offset:9216\nv_mfma_f32_16x16x32_bf16 v[36:39], v[84:87], v[72:75], v[36:39]\n" \
  "ds_read_b128 v[136:139], %[x1h] offset:4096\nds_read_b64_tr_b16 v[152:153], %[w2h] offset:8192\nv_mfma_f32_16x16x32_bf16 v[40:43], v[88:91], v[72:75], v[40:43]\n" \
  "ds_read_b64_tr_b16 v[154:155], %[w2h] offset:9216\nds_read_b128 v[140:143], %[x1h] offset:6144\nv_mfma_f32_16x16x32_bf16 v[44:47], v[92:95], v[72:75], v[44:47]\n" \
  "ds_read_b64_tr_b16 v[156:157], %[w3h] offset:8192\nds_read_b64_tr_b16 v[158:159], %[w3h] offset:9216\nv_mfma_f32_16x16x32_bf16 v[48:51], v[80:83], v[76:79], v[48:51]\n" \
  "v_mfma_f32_16x16x32_bf16 v[52:55], v[84:87], v[76:79], v[52:55]\nv_mfma_f32_16x16x32_bf16 v[56:59], v[88:91], v[76:79], v[56:59]\n" \
  "v_mfma_f32_16x16x32_bf16 v[60:63], v[92:95], v[76:79], v[60:63]\ns_add_u32 s42, s42, 1\ns_waitcnt lgkmcnt(12)\n" \
  "v_mfma_f32_16x16x32_bf16 v[0:3], v[112:115], v[96:99], v[0:3]\nv_mfma_f32_16x16x32_bf16 v[4:7], v[116:119], v[96:99], v[4:7]\n" \
  "v_mfma_f32_16x16x32_bf16 v[8:11], v[120:123], v[96:99], v[8:11]\nv_mfma_f32_16x16x32_bf16 v[12:15], v[124:127], v[96:99], v[12:15]\n" \
  "v_mfma_f32_16x16x32_bf16 v[16:19], v[112:115], v[100:103], v[16:19]\nv_mfma_f32_16x16x32_bf16 v[20:23], v[116:119], v[100:103], v[20:23]\n" \
  "v_mfma_f32_16x16x32_bf16 v[24:27], v[120:123], v[100:103], v[24:27]\nv_mfma_f32_16x16x32_bf16 v[28:31], v[124:127], v[100:103], v[28:31]\n" \
  "v_mfma_f32_16x16x32_bf16 v[32:35], v[112:115], v[104:107], v[32:35]\nv_mfma_f32_16x16x32_bf16 v[36:39], v[116:119], v[104:107], v[36:39]\n" \
  "v_mfma_f32_16x16x32_bf16 v[40:43], v[120:123], v[104:107], v[40:43]\nv_mfma_f32_16x16x32_bf16 v[44:47], v[124:127], v[104:107], v[44:47]\n" \
  "v_mfma_f32_16x16x32_bf16 v[48:51], v[112:115], v[108:111], v[48:51]\nv_mfma_f32_16x16x32_bf16 v[52:55], v[116:119], v[108:111], v[52:55]\n" \
  "v_mfma_f32_16x16x32_bf16 v[56:59], v[120:123], v[108:111], v[56:59]\nv_mfma_f32_16x16x32_bf16 v[60:63], v[124:127], v[108:111], v[60:63]\ns_waitcnt lgkmcnt(0)\n" \
  "s_barrier\n" GT2 "s_cmp_eq_u32 s42, 6\ns_cbranch_scc0 3f\nbuffer_load_dwordx2 %[fu], %[fa], %[ws], 0 offen sc1\n3:\n" \
  "v_mfma_f32_16x16x32_bf16 v[0:3], v[144:147], v[128:131], v[0:3]\nds_read_b128 v[64:67], %[x0] offset:0\nds_read_b64_tr_b16 v[80:81], %[w0] offset:0\n" \
  "v_mfma_f32_16x16x32_bf16 v[4:7], v[148:151], v[128:131], v[4:7]\nds_read_b64_tr_b16 v[82:83], %[w0] offset:1024\nds_read_b128 v[68:71], %[x0] offset:2048\n" \
  "v_mfma_f32_16x16x32_bf16 v[8:11], v[152:155], v[128:131], v[8:11]\nds_read_b64_tr_b16 v[84:85], %[w1] offset:0\nds_read_b64_tr_b16 v[86:87], %[w1] offset:1024\n" \
  "v_mfma_f32_16x16x32_bf16 v[12:15], v[156:159], v[128:131], v[12:15]\nds_read_b128 v[72:75], %[x0] offset:4096\nds_read_b64_tr_b16 v[88:89], %[w2] offset:0\n" \
  "v_mfma_f32_16x16x32_bf16 v[16:19], v[144:147], v[132:135], v[16:19]\nds_read_b64_tr_b16 v[90:91], %[w2] offset:1024\nds_read_b128 v[76:79], %[x0] offset:6144\n" \
  "v_mfma_f32_16x16x32_bf16 v[20:23], v[148:151], v[132:135], v[20:23]\nds_read_b64_tr_b16 v[92:93], %[w3] offset:0\nds_read_b64_tr_b16 v[94:95], %[w3] offset:1024\n" \
  "v_mfma_f32_16x16x32_bf16 v[24:27], v[152:155], v[132:135], v[24:27]\nds_read_b128 v[96:99], %[x1] offset:0\nds_read_b64_tr_b16 v[112:113], %[w0] offset:8192\n" \
  "v_mfma_f32_16x16x32_bf16 v[28:31], v[156:159], v[132:135], v[28:31]\nds_read_b64_tr_b16 v[114:115], %[w0] offset:9216\nds_read_b128 v[100:103], %[x1] offset:2048\n" \
  "v_mfma_f32_16x16x32_bf16 v[32:35], v[144:147], v[136:139], v[32:35]\nds_read_b64_tr_b16 v[116:117], %[w1] offset:8192\nds_read_b64_tr_b16 v[118:119], %[w1] offset:9216\n" \
  "v_mfma_f32_16x16x32_bf16 v[36:39], v[148:151], v[136:139], v[36:39]\nds_read_b128 v[104:107], %[x1] offset:4096\nds_read_b64_tr_b16 v[120:121], %[w2] offset:8192\n" \
  "v_mfma_f32_16x16x32_bf16 v[40:43], v[152:155], v[136:139], v[40:43]\nds_read_b64_tr_b16 v[122:123], %[w2] offset:9216\nds_read_b128 v[108:111], %[x1] offset:6144\n" \
  "v_mfma_f32_16x16x32_bf16 v[44:47], v[156:159], v[136:139], v[44:47]\nds_read_b64_tr_b16 v[124:125], %[w3] offset:8192\nds_read_b64_tr_b16 v[126:127], %[w3] offset:9216\n" \
  "v_mfma_f32_16x16x32_bf16 v[48:51], v[144:147], v[140:143], v[48:51]\nv_mfma_f32_16x16x32_bf16 v[52:55], v[148:151], v[140:143], v[52:55]\n" \
  "v_mfma_f32_16x16x32_bf16 v[56:59], v[152:155], v[140:143], v[56:59]\nv_mfma_f32_16x16x32_bf16 v[60:63], v[156:159], v[140:143], v[60:63]\ns_add_u32 s42, s42, 1\n" \
  "s_waitcnt lgkmcnt(12)\ns_branch 1b\n9:\ns_waitcnt lgkmcnt(0)\nv_mfma_f32_16x16x32_bf16 v[0:3], v[112:115], v[96:99], v[0:3]\n" \
  "v_mfma_f32_16x16x32_bf16 v[4:7], v[116:119], v[96:99], v[4:7]\nv_mfma_f32_16x16x32_bf16 v[8:11], v[120:123], v[96:99], v[8:11]\n" \
  "v_mfma_f32_16x16x32_bf16 v[12:15], v[124:127], v[96:99], v[12:15]\nv_mfma_f32_16x16x32_bf16 v[16:19], v[112:115], v[100:103], v[16:19]\n" \
  "v_mfma_f32_16x16x32_bf16 v[20:23], v[116:119], v[100:103], v[20:23]\nv_mfma_f32_16x16x32_bf16 v[24:27], v[120:123], v[100:103], v[24:27]\n" \
  "v_mfma_f32_16x16x32_bf16 v[28:31], v[124:127], v[100:103], v[28:31]\nv_mfma_f32_16x16x32_bf16 v[32:35], v[112:115], v[104:107], v[32:35]\n" \
  "v_mfma_f32_16x16x32_bf16 v[36:39], v[116:119], v[104:107], v[36:39]\nv_mfma_f32_16x16x32_bf16 v[40:43], v[120:123], v[104:107], v[40:43]\n" \
  "v_mfma_f32_16x16x32_bf16 v[44:47], v[124:127], v[104:107], v[44:47]\nv_mfma_f32_16x16x32_bf16 v[48:51], v[112:115], v[108:111], v[48:51]\n" \
  "v_mfma_f32_16x16x32_bf16 v[52:55], v[116:119], v[108:111], v[52:55]\nv_mfma_f32_16x16x32_bf16 v[56:59], v[120:123], v[108:111], v[56:59]\n" \
  "v_mfma_f32_16x16x32_bf16 v[60:63], v[124:127], v[108:111], v[60:63]\ns_waitcnt vmcnt(0)\ns_nop 7\n"
#define LOAD_ASM \
  "s_mov_b64 s[40:41], %[xb]\ns_mov_b64 s[44:45], %[wb]\ns_add_u32 s42, s40, 81920\ns_addc_u32 s43, s41, 0\ns_add_u32 s46, s44, 122880\ns_addc_u32 s47, s45, 0\n" \
  "s_mov_b32 s48, 32768\ns_mov_b32 s49, 1\n1:\ns_add_u32 m0, s48, 20480\ns_nop 0\nglobal_load_lds_dwordx4 v8, s[44:45] offset:-4096\n" \
  "global_load_lds_dwordx4 v9, s[44:45] offset:-3072\nglobal_load_lds_dwordx4 v10, s[44:45] offset:-2048\nglobal_load_lds_dwordx4 v11, s[44:45] offset:-1024\n" \
  "global_load_lds_dwordx4 v12, s[44:45] offset:0\nglobal_load_lds_dwordx4 v13, s[44:45] offset:1024\nglobal_load_lds_dwordx4 v14, s[44:45] offset:2048\n" \
  "global_load_lds_dwordx4 v15, s[44:45] offset:3072\ns_add_u32 m0, s48, 4096\ns_nop 0\nglobal_load_lds_dwordx4 v0, s[40:41] offset:-4096\n" \
  "global_load_lds_dwordx4 v1, s[40:41] offset:-3072\nglobal_load_lds_dwordx4 v2, s[40:41] offset:-2048\nglobal_load_lds_dwordx4 v3, s[40:41] offset:-1024\n" \
  "global_load_lds_dwordx4 v4, s[40:41] offset:0\nglobal_load_lds_dwordx4 v5, s[40:41] offset:1024\nglobal_load_lds_dwordx4 v6, s[40:41] offset:2048\n" \
  "global_load_lds_dwordx4 v7, s[40:41] offset:3072\ns_add_u32 m0, s48, 28672\ns_nop 0\nglobal_load_lds_dwordx4 v8, s[46:47] offset:-4096\n" \
  "global_load_lds_dwordx4 v9, s[46:47] offset:-3072\nglobal_load_lds_dwordx4 v10, s[46:47] offset:-2048\nglobal_load_lds_dwordx4 v11, s[46:47] offset:-1024\n" \
  "global_load_lds_dwordx4 v12, s[46:47] offset:0\nglobal_load_lds_dwordx4 v13, s[46:47] offset:1024\nglobal_load_lds_dwordx4 v14, s[46:47] offset:2048\n" \
  "global_load_lds_dwordx4 v15, s[46:47] offset:3072\ns_add_u32 m0, s48, 12288\ns_nop 0\nglobal_load_lds_dwordx4 v0, s[42:43] offset:-4096\n" \
  "global_load_lds_dwordx4 v1, s[42:43] offset:-3072\nglobal_load_lds_dwordx4 v2, s[42:43] offset:-2048\nglobal_load_lds_dwordx4 v3, s[42:43] offset:-1024\n" \
  "global_load_lds_dwordx4 v4, s[42:43] offset:0\nglobal_load_lds_dwordx4 v5, s[42:43] offset:1024\nglobal_load_lds_dwordx4 v6, s[42:43] offset:2048\n" \
  "global_load_lds_dwordx4 v7, s[42:43] offset:3072\ns_add_u32 s40, s40, 128\ns_addc_u32 s41, s41, 0\ns_add_u32 s42, s42, 128\ns_addc_u32 s43, s43, 0\n" \
  "s_add_u32 s44, s44, 245760\ns_addc_u32 s45, s45, 0\ns_add_u32 s46, s46, 245760\ns_addc_u32 s47, s47, 0\ns_add_u32 s48, s48, 32768\ns_cmp_eq_u32 s48, 98304\n" \
  "s_cselect_b32 s48, 0, s48\ns_waitcnt vmcnt(32)\ns_barrier\ns_add_u32 s49, s49, 1\ns_cmp_lt_u32 s49, 10\ns_cbranch_scc1 1b\ns_waitcnt vmcnt(0)\ns_barrier\n"
typedef unsigned u32x16 __attribute__((ext_vector_type(16)));
typedef unsigned u32x8 __attribute__((ext_vector_type(8)));
DI unsigned wave_max_u32(unsigned m) {
  m = max(m, (unsigned)__builtin_amdgcn_update_dpp(0, (int)m, 0x111, 0xf, 0xf, true));
  m = max(m, (unsigned)__builtin_amdgcn_update_dpp(0, (int)m, 0x112, 0xf, 0xf, true));
  m = max(m, (unsigned)__builtin_amdgcn_update_dpp(0, (int)m, 0x114, 0xf, 0xf, true));
  m = max(m, (unsigned)__builtin_amdgcn_update_dpp(0, (int)m, 0x118, 0xf, 0xf, true));
  return max(max((unsigned)__builtin_amdgcn_readlane((int)m, 15), (unsigned)__builtin_amdgcn_readlane((int)m, 31)),
             max((unsigned)__builtin_amdgcn_readlane((int)m, 47), (unsigned)__builtin_amdgcn_readlane((int)m, 63)));
}
DI void stage_u(int c, int lane, const u32x4 (&v)[4]) {
  unsigned m2 = 0;
#pragma unroll
  for (int i = 0; i < 4; i++)
#pragma unroll
    for (int j = 0; j < 4; j++) asm("v_pk_maximum3_f16 %0, %0, %1, %1 neg_lo:[0,0,1] neg_hi:[0,0,1]" : "+v"(m2) : "v"(v[i][j]));
  m2 &= 0x7fff7fffu;
  const int B = max((int)(wave_max_u32(max(m2 & 0xffffu, m2 >> 16)) >> 7) - 30, 0);
  u16x2 b2; b2[0] = b2[1] = (unsigned short)(B << 7);
  const unsigned ub = L_U + c * U_BYTES;
#pragma unroll
  for (int i = 0; i < 4; i++) {
    u32x4 w;
#pragma unroll
    for (int j = 0; j < 4; j++) {
      const u16x2 t = __builtin_elementwise_sub_sat(__builtin_bit_cast(u16x2, v[i][j] & 0x7fff7fffu), b2);
      w[j] = (__builtin_bit_cast(unsigned, t) << 3) | (v[i][j] & 0x80008000u);
    }
    ldsw<u32x4>(ub + 512 + i * 1024 + lane * 16, w);
  }
  if (lane < 32) ldsw<u32x4>(ub + lane * 16, u32x4{0, 0, 0, 0});
  if (lane == 0) ldsw<int>(L_MISC + 32 + c * 4, B - 112);
}

struct Pre { unsigned wa[8], ww, fq, ca, cb, aV, aV2, ylo; u32x8 vw; };
DI void precompute(Pre& r) {
  const int tid = threadIdx.x, lane = tid & 63, wave = __builtin_amdgcn_readfirstlane(tid >> 6), b = blockIdx.x;
  const int x0 = 4 * tid - 3;
#pragma unroll
  for (int i = 0; i < 8; i += 2) {
    const unsigned t = pk_bf16((float)x0 + (float)i, (float)x0 + (float)(i + 1));
    asm("v_mad_u32_u16 %0, %1, 2, %2" : "=v"(r.wa[i]) : "v"(t), "s"(L_KC - 2 * (0x4300 - 128)));
    asm("v_mad_u32_u16 %0, %1, 2, %2 op_sel:[1,0,0,0]" : "=v"(r.wa[i + 1]) : "v"(t), "s"(L_KC - 2 * (0x4300 - 128)));
  }
  if (wave == 0) {
#pragma unroll
    for (int i = 0; i < 7; i++) r.wa[i] = L_KC + (x0 + i < 0 ? 672 : x0 + i) * 2;
  }
  r.ww = L_W4 + 128 + tid * 32;
  const int head = (b & 7) * 4 + (b >> 6), chunk = (b >> 3) & 7;
  r.fq = F_Q + ((lane & (lane < 32 ? 8 : 1) ? (20 * head + 19) >> 7 : (20 * head) >> 7) + (lane < 16 ? 5 : lane < 32 ? 10 : 0)) * 128 + (lane < 32 ? lane & 7 : chunk) * 16;
  r.ca = L_W4 + (28 + (lane & 15) - 8 * (lane >> 4)) * 8; r.cb = L_U + (240 + 16 * (lane & 15) + 8 * (lane >> 4)) * 2 - 128;
  r.ylo = (lane & 15) * 32 + (lane >> 4) * 8;
  { const unsigned h = lane >> 5, rr = (lane >> 2) & 3, c = lane & 3; const bool blk = lane & 16;
    r.aV = (wave >> 2) * PAIR + v_off(blk ? 2 : c >> 1, 8 * h + rr) + (blk ? min(c, 1u) : c & 1) * 8;
    r.aV2 = r.aV ^ 64; }
#pragma unroll
  for (int k = 0; k < 5; k++) r.vw[k] = (((lane + 64 * k) * 6554) >> 16) * 1120 + lane * 16 + 3072;
  r.vw[5] = (lane >> 3) * 4096 + (lane & 7) * 16; r.vw[6] = r.vw[7] = 0;
  asm volatile("" : "+v"(r.wa[0]), "+v"(r.wa[1]), "+v"(r.wa[2]), "+v"(r.wa[3]), "+v"(r.wa[4]), "+v"(r.wa[5]), "+v"(r.wa[6]), "+v"(r.ww), "+v"(r.fq),
               "+v"(r.ca), "+v"(r.cb), "+v"(r.aV), "+v"(r.aV2), "+v"(r.ylo), "+v"(r.vw));
}
DI void touch(const char* a, unsigned& d) { asm volatile("global_load_dword %0, %1, off" : "+v"(d) : "v"(a)); }
DI void loader(const P& p, rsrc_t ws, int rb, int cg, int rj, int cj, int ch0, int lane) {
  {
    set_m0(L_PAR + 3072);
    if (lane < 48) {
      const unsigned po = lane * 16;
      dma<-3072, false>(p.lar + ch0 * 256 + 3072, po); dma<-2304, false>(p.aim + ch0 * 256 + 2304, po);
      dma<-1536, false>(p.cre + ch0 * 256 + 1536, po); dma<-768, false>(p.cim + ch0 * 256 + 768, po);
      asm volatile("global_load_lds_ushort %0, %1" :: "v"(min(lane, 2) * 2), "s"(p.ldt + ch0 * 2));
    }
    set_m0(L_MISC);
    if (lane < 2) asm volatile("global_load_lds_dword %0, %1 sc1" :: "v"(lane * 4), "s"(p.ws + F_E));
  }
  {
    const char* ub = p.u + rj * 64 * 1280 + cj * 160;
    unsigned vu[5];
#pragma unroll
    for (int i = 0; i < 5; i++) { const int n = i * 64 + lane; vu[i] = (n / 10) * 1280 + (n % 10) * 16 + 2048 - i * 1024; }
#pragma unroll
    for (int t = 0; t < 2; t++) {
      set_m0(L_U + t * 5120 + 2048);
      const char* s = ub + t * 32 * 1280;
      dma<-2048, false>(s, vu[0]); dma<-1024, false>(s, vu[1]); dma<0, false>(s, vu[2]); dma<1024, false>(s, vu[3]); dma<2048, false>(s, vu[4]);
    }
  }
  u32x8 vx, vw;
  const unsigned r = lane >> 3, c = lane & 7, q = lane >> 4, qs = min(cg, 14) % 5 == 3 ? q : 2 * q;
#pragma unroll
  for (int i = 0; i < 8; i++) {
    vx[i] = r * 1280 + ((c ^ (4 * (i & 1) + (r >> 1))) << 4) + i * (8 * 1280 - 1024);
    vw[i] = q * 3840 + (((lane & 15) ^ (qs + 4 * (i & 2))) << 4) + i * (4 * 3840 - 1024);
  }
  const char* xb = p.x + rb * 128 * 1280 + 4096;
  const char* wb = p.wqkv + min(cg, 14) * 256 + 4096;
  auto group = [&](const char* b, const u32x8& v, unsigned m) {
    set_m0(m + 4096);
    dma<-4096, false>(b, v[0]); dma<-3072, false>(b, v[1]); dma<-2048, false>(b, v[2]); dma<-1024, false>(b, v[3]);
    dma<0, false>(b, v[4]); dma<1024, false>(b, v[5]); dma<2048, false>(b, v[6]); dma<3072, false>(b, v[7]);
  };
  group(wb, vw, 16384); group(xb, vx, 0); group(wb + 32 * 3840, vw, 24576); group(xb + 64 * 1280, vx, 8192);
  WAITV(32);
  bar();
  asm volatile(LOAD_ASM
               :: "{v[0:7]}"(vx), "{v[8:15]}"(vw), [xb] "s"(xb + 128), [wb] "s"(wb + 64 * 3840)
               : "memory", "scc", "s40", "s41", "s42", "s43", "s44", "s45", "s46", "s47", "s48", "s49");
  STAMP(42);
}

DI void gemm_phase(const P& p, rsrc_t ws, int rb, int cg, int rj, int cj, int ch0, int wave, int lane, Pre& pre, u32x2& tag) {
  unsigned sink = 0;
  if (wave == 2) {
    const char* pc; unsigned len;
    asm volatile("s_getpc_b64 %0\n 1:\n s_mov_b32 %1, .Lfused_last-1b" : "=s"(pc), "=s"(len));
    touch(pc + min(((blockIdx.x >> 3) * 16 + (lane & 15)) * 128, len - 4), sink);
  }
  const int wm = wave >> 1, wn = wave & 1, g = lane >> 4, tk = lane & 15;
  const unsigned ax0 = (wm * 64 + tk) * 128 + ((g ^ (tk >> 1)) << 4); unsigned aw[4];
  const int c5 = cg % 5, ty = cg / 5;
  const unsigned long long tp = c5 == 0 ? 0xf6f34b45ec51ca0ull : c5 == 1 ? 0xf7337a49ed520a3ull : c5 == 2 ? 0xe6af5941ab41861ull : c5 == 3 ? 0xeeab0c4d6672481ull : 0xeef169c5cc49c82ull;
  const unsigned long long tsg = c5 == 0 ? 0xee26e49062ull : c5 == 1 ? 0xdda2c38820ull : c5 == 2 ? 0xffb347a8a0ull : c5 == 3 ? 0xe5e4dfa060ull : 0xfeab059820ull;
  auto quad = [&](int T, unsigned n) { return T < 6 ? ((unsigned)(tp >> (20 * (T >> 1) + 5 * n)) & 31) + (T & 1) : (unsigned)(tsg >> (20 * (T - 6) + 5 * n)) & 31; };
#pragma unroll
  for (int f = 0; f < 4; f++) {
    const unsigned u = wn ? quad(4 + f, lane & 3) : quad(f, lane & 3), q = (lane >> 2) & 3;
    aw[f] = 16384 + g * 2048 + q * 256 + (((u >> 1) ^ ((c5 == 3 ? q : 2 * q) + 8 * (g & 1))) << 4) + (u & 1) * 8;
  }
  f32x16 acc[4];
#pragma unroll
  for (int a = 0; a < 4; a++)
#pragma unroll
    for (int i = 0; i < 16; i++) acc[a][i] = 0.f;
  unsigned ea[3][4]; bool vt[3];
#pragma unroll
  for (int e = 0; e < 3; e++) {
    const unsigned u = 32 * c5 + (wn ? quad(e ? 5 + e : 4, g) : quad(2 * min(e, 1), g)), hd = (u * 205) >> 10, c = u - 5 * hd, t0 = rb * 4 + wm * 2, pr = t0 >> 1;
    const unsigned img = ty == 0 ? WS_Q + (hd * 64 + t0) * QT : WS_KV + hd * 16 * POS + (pr & 15) * POS + (pr >> 4) * PAIR + (ty == 2 ? 2560 : 0);
    const bool tail = c == 4;
    const unsigned base = img + (ty == 2 ? v_off(c >> 1, tk) + (c & 1) * 8
                                         : (tail ? (ty ? 2048 : 1024) + tk * 8 : tk * 16 + (c >> 1) * 512 + (c & 1) * 8));
    const unsigned s16 = ty == 2 ? 768 : tail ? 128 : 256, s32 = ty == 0 ? QT : ty == 2 ? 1536 : tail ? 256 : 1024;
#pragma unroll
    for (int tt = 0; tt < 4; tt++) ea[e][tt] = (base + (tt & 1) * s16 + (tt >> 1) * s32) | (cg == 15 ? 0x80000000u : 0);
    vt[e] = tail;
    asm volatile("" : "+v"(ea[e][0]), "+v"(ea[e][1]), "+v"(ea[e][2]), "+v"(ea[e][3]));
  }
  precompute(pre);
  bar();
  {
    const unsigned a = L_U + ((2 * g + ((lane >> 1) & 1)) * 8 + ((lane >> 2) & 3)) * 160 + wave * 16 + (lane & 1) * 8, w = L_W4 + (wave < 3 ? wave * W4_BYTES + 26 * 256 : 11 * 256);
    u32x4 t[3];
#pragma unroll
    for (int i = 0; i < 3; i++) t[i] = cat(ldtr(a + i * 64), ldtr(a + i * 64 + 4 * 160));
#pragma unroll
    for (int i = 0; i < 3; i++) { ldsw<u32x4>(w + (lane & 7) * 144 + (lane >> 3) * 16, t[i]); t[i] = ldsr<u32x4>(w + (lane >> 3) * 144 + (lane & 7) * 16); }
#pragma unroll
    for (int i = 0; i < 3; i++) if (i < 2 || wave < 2) st16s(ws, (lane >> 3) * 4096 + (lane & 7) * 16, WS_UT + (cj * 80 + (wave + 4 * i) * 8) * 4096 + rj * 128, t[i]);
  }
  tag = call_tag();
  const int ch = ch0 + min(wave, 2);
  const unsigned fa = F_U + ((blockIdx.x >> 5) * 32 + (lane & 31)) * 8;
  u32x2 fu; u32x16 ur; int ok;
  bar();
  asm volatile(GEMM_ASM
      : "+{v[0:15]}"(acc[0]), "+{v[16:31]}"(acc[1]), "+{v[32:47]}"(acc[2]), "+{v[48:63]}"(acc[3]), "+v"(sink), [fu] "=&v"(fu), "=&{v[160:175]}"(ur), [ok] "=&s"(ok)
      : [x0] "v"(ax0), [x1] "v"(ax0 ^ 64), [w0] "v"(aw[0]), [w1] "v"(aw[1]), [w2] "v"(aw[2]), [w3] "v"(aw[3]),
        [x0h] "v"(ax0 + 65536), [x1h] "v"((ax0 ^ 64) + 65536), [w0h] "v"(aw[0] + 65536), [w1h] "v"(aw[1] + 65536), [w2h] "v"(aw[2] + 65536), [w3h] "v"(aw[3] + 65536),
        [pub] "s"(wave ? 0 : 2), [tag] "v"(tag), [ws] "s"(ws), [fo] "s"(F_U + (cj * 32 + rj) * 8), [fa] "v"(fa), [ua] "v"(WS_UT + ch * 4096 + lane * 16)
      : "memory", "scc", "vcc", "s42", "v64", "v65", "v66", "v67", "v68", "v69", "v70", "v71", "v72", "v73", "v74", "v75", "v76", "v77", "v78", "v79", "v80", "v81", "v82", "v83", "v84", "v85", "v86", "v87", "v88", "v89", "v90", "v91", "v92", "v93", "v94", "v95", "v96", "v97", "v98", "v99", "v100", "v101", "v102", "v103", "v104", "v105", "v106", "v107", "v108", "v109", "v110", "v111", "v112", "v113", "v114", "v115", "v116", "v117", "v118", "v119", "v120", "v121", "v122", "v123", "v124", "v125", "v126", "v127", "v128", "v129", "v130", "v131", "v132", "v133", "v134", "v135", "v136", "v137", "v138", "v139", "v140", "v141", "v142", "v143", "v144", "v145", "v146", "v147", "v148", "v149", "v150", "v151", "v152", "v153", "v154", "v155", "v156", "v157", "v158", "v159");
  STAMP(2);
  u32x4 uv[4];
  if (ok) {
#pragma unroll
    for (int i = 0; i < 16; i++) uv[i >> 2][i & 3] = ur[i];
  } else {
    tags_wait(ws, fa, tag);
#pragma unroll
    for (int i = 0; i < 4; i++) uv[i] = ld16(ws, WS_UT + ch * 4096 + i * 1024 + lane * 16);
    asm volatile("" : "+v"(uv[0]), "+v"(uv[1]), "+v"(uv[2]), "+v"(uv[3]));
  }
  if (ty == 0) {
#pragma unroll
    for (int tt = 0; tt < 4; tt++) acc[tt] *= 0.32259784f;
  }
#pragma unroll
  for (int tt = 0; tt < 4; tt++) st16(ws, ea[0][tt], u32x4{pk_bf16(acc[tt][0], acc[tt][1]), pk_bf16(acc[tt][2], acc[tt][3]), pk_bf16(acc[tt][4], acc[tt][5]), pk_bf16(acc[tt][6], acc[tt][7])});
  if (wn == 0) {
#pragma unroll
    for (int tt = 0; tt < 4; tt++) st16(ws, ea[1][tt], u32x4{pk_bf16(acc[tt][8], acc[tt][9]), pk_bf16(acc[tt][10], acc[tt][11]), pk_bf16(acc[tt][12], acc[tt][13]), pk_bf16(acc[tt][14], acc[tt][15])});
  } else if (ty != 2) {
#pragma unroll
    for (int f = 0; f < 2; f++)
#pragma unroll
      for (int tt = 0; tt < 4; tt++) st8(ws, ea[1 + f][tt], u32x2{pk_bf16(acc[tt][8 + 4 * f], acc[tt][9 + 4 * f]), pk_bf16(acc[tt][10 + 4 * f], acc[tt][11 + 4 * f])});
  } else {
#pragma unroll
    for (int f = 0; f < 2; f++) {
      u32x2 w[4];
#pragma unroll
      for (int tt = 0; tt < 4; tt++) w[tt] = u32x2{pk_bf16(acc[tt][8 + 4 * f], acc[tt][9 + 4 * f]), pk_bf16(acc[tt][10 + 4 * f], acc[tt][11 + 4 * f])};
      if (vt[1 + f]) {
#pragma unroll
        for (int tt = 0; tt < 4; tt++) st16(ws, ea[1 + f][tt], u32x4{w[tt][0], w[tt][1], 0x00003f80u, 0});
      } else {
#pragma unroll
        for (int tt = 0; tt < 4; tt++) st8(ws, ea[1 + f][tt], w[tt]);
      }
    }
  }
  STAMP(3);
  if (wave < 3) stage_u(wave, lane, uv);
}

#define KGEN_ASM \
  "v_mul_f32 v41, 0x41000000, %[ai0]\nv_mul_f32 v37, 0x41000000, %[a20]\nv_mul_f32 v42, 0x43000000, %[ai0]\nv_mul_f32 v38, 0x43000000, %[a20]\n" \
  "v_mul_f32 v43, 0x43800000, %[ai0]\nv_mul_f32 v39, 0x43800000, %[a20]\nv_fract_f32 v40, %[ai0]\nv_fract_f32 v41, v41\nv_fract_f32 v42, v42\nv_fract_f32 v43, v43\n" \
  "v_exp_f32 v36, %[a20]\nv_exp_f32 v37, v37\nv_exp_f32 v38, v38\nv_exp_f32 v39, v39\nv_cos_f32 v44, v40\nv_sin_f32 v48, v40\nv_cos_f32 v45, v41\nv_sin_f32 v49, v41\n" \
  "v_cos_f32 v46, v42\nv_sin_f32 v50, v42\nv_cos_f32 v47, v43\nv_sin_f32 v51, v43\nv_mov_b32 v52, 0x3c00\nv_mul_f32 v0, v36, v44\nv_mul_f32 v1, v36, v48\n" \
  "v_mul_f32 v2, v36, -v48\nv_mul_f32 v3, v37, v45\nv_mul_f32 v4, v37, v49\nv_mul_f32 v5, v37, -v49\nv_mul_f32 v20, v38, v46\nv_mul_f32 v21, v38, v50\n" \
  "v_mul_f32 v6, v39, v47\nv_mul_f32 v7, v39, v51\nv_mul_f32 v8, v39, -v51\nv_mul_f32 v9, v1, v2\nv_mul_f32 v10, v1, v0\nv_mul_f32 v28, %[ci0], v8\n" \
  "v_mul_f32 v29, %[ci0], v6\nv_mul_f32 v26, %[ci0], v2\nv_mul_f32 v27, %[ci0], v0\nv_cvt_pk_f16_f32 v53, v20, v21\nv_cvt_pk_f16_f32 v54, %[cr0], -%[ci0]\n" \
  "v_fmac_f32 v9, v0, v0\nv_fmac_f32 v10, v0, v1\nv_fmac_f32 v28, %[cr0], v6\nv_fmac_f32 v29, %[cr0], v7\nv_fmac_f32 v26, %[cr0], v0\nv_fmac_f32 v27, %[cr0], v1\n" \
  "ds_write2st64_b32 v64, v52, v53 offset0:32 offset1:48\nv_xor_b32 v11, 0x80000000, v10\nv_mul_f32 v22, v21, v5\nv_mul_f32 v23, v21, v3\nv_mul_f32 v12, v10, v11\n" \
  "v_mul_f32 v13, v10, v9\nv_mul_f32 v32, v29, v8\nv_mul_f32 v33, v29, v6\nv_mul_f32 v30, v29, v11\nv_mul_f32 v31, v29, v9\nv_cvt_pk_f16_f32 v55, v28, -v29\n" \
  "v_fmac_f32 v22, v20, v3\nv_fmac_f32 v23, v20, v4\nv_fmac_f32 v12, v9, v9\nv_fmac_f32 v13, v9, v10\nv_fmac_f32 v32, v28, v6\nv_fmac_f32 v33, v28, v7\n" \
  "v_fmac_f32 v30, v28, v9\nv_fmac_f32 v31, v28, v10\nds_write2st64_b32 v64, v54, v55 offset0:0 offset1:16\nv_xor_b32 v14, 0x80000000, v13\nv_mul_f32 v16, v4, v5\n" \
  "v_mul_f32 v17, v4, v3\nv_mul_f32 v20, v23, v5\nv_mul_f32 v21, v23, v3\nv_mul_f32 v24, v27, v2\nv_mul_f32 v25, v27, v0\nv_mul_f32 v28, v31, v11\nv_mul_f32 v29, v31, v9\n" \
  "v_cvt_pk_f16_f32 v52, v3, v4\nv_cvt_pk_f16_f32 v53, v22, v23\nv_cvt_pk_f16_f32 v54, v26, -v27\nv_cvt_pk_f16_f32 v55, v30, -v31\nv_fmac_f32 v16, v3, v3\n" \
  "v_fmac_f32 v17, v3, v4\ns_barrier\nv_fmac_f32 v20, v22, v3\nv_fmac_f32 v21, v22, v4\nv_fmac_f32 v24, v26, v0\nv_fmac_f32 v25, v26, v1\nv_fmac_f32 v28, v30, v9\n" \
  "v_fmac_f32 v29, v30, v10\nds_write2st64_b32 v65, v52, v53 offset0:33 offset1:49\nds_write2st64_b32 v65, v54, v55 offset0:1 offset1:17\nv_mul_f32 v18, v17, v5\n" \
  "v_mul_f32 v19, v17, v3\nv_mul_f32 v22, v21, v5\nv_mul_f32 v23, v21, v3\nv_mul_f32 v26, v25, v2\nv_mul_f32 v27, v25, v0\nv_mul_f32 v30, v29, v11\nv_mul_f32 v31, v29, v9\n" \
  "v_cvt_pk_f16_f32 v52, v16, v17\nv_cvt_pk_f16_f32 v53, v20, v21\nv_cvt_pk_f16_f32 v54, v24, -v25\nv_cvt_pk_f16_f32 v55, v28, -v29\nv_fmac_f32 v18, v16, v3\n" \
  "v_fmac_f32 v19, v16, v4\nv_fmac_f32 v22, v20, v3\nv_fmac_f32 v23, v20, v4\nv_fmac_f32 v26, v24, v0\nv_fmac_f32 v27, v24, v1\nv_fmac_f32 v30, v28, v9\n" \
  "v_fmac_f32 v31, v28, v10\nds_write2st64_b32 v66, v52, v53 offset0:34 offset1:50\nds_write2st64_b32 v66, v54, v55 offset0:2 offset1:18\nv_mul_f32 v16, v19, v5\n" \
  "v_mul_f32 v17, v19, v3\nv_mul_f32 v20, v23, v5\nv_mul_f32 v21, v23, v3\nv_mul_f32 v24, v27, v2\nv_mul_f32 v25, v27, v0\ns_barrier\nv_cvt_pk_f16_f32 v52, v18, v19\n" \
  "v_cvt_pk_f16_f32 v53, v22, v23\nv_cvt_pk_f16_f32 v54, v26, -v27\nv_cvt_pk_f16_f32 v55, v30, -v31\nv_fmac_f32 v16, v18, v3\nv_fmac_f32 v17, v18, v4\n" \
  "v_fmac_f32 v20, v22, v3\nv_fmac_f32 v21, v22, v4\nv_fmac_f32 v24, v26, v0\nv_fmac_f32 v25, v26, v1\nds_write2st64_b32 v67, v52, v53 offset0:35 offset1:51\n" \
  "ds_write2st64_b32 v67, v54, v55 offset0:3 offset1:19\nv_mul_f32 v18, v17, v5\nv_mul_f32 v19, v17, v3\nv_mul_f32 v22, v21, v5\nv_mul_f32 v23, v21, v3\n" \
  "v_mul_f32 v26, v25, v2\nv_mul_f32 v27, v25, v0\nv_mul_f32 v28, v33, v14\nv_mul_f32 v29, v33, v12\nv_cvt_pk_f16_f32 v52, v16, v17\nv_cvt_pk_f16_f32 v53, v20, v21\n" \
  "v_cvt_pk_f16_f32 v54, v24, -v25\nv_cvt_pk_f16_f32 v55, v32, -v33\nv_fmac_f32 v18, v16, v3\nv_fmac_f32 v19, v16, v4\nv_fmac_f32 v22, v20, v3\nv_fmac_f32 v23, v20, v4\n" \
  "v_fmac_f32 v26, v24, v0\nv_fmac_f32 v27, v24, v1\nv_fmac_f32 v28, v32, v12\nv_fmac_f32 v29, v32, v13\nds_write2st64_b32 v68, v52, v53 offset0:36 offset1:52\n" \
  "ds_write2st64_b32 v68, v54, v55 offset0:4 offset1:20\nv_mul_f32 v16, v19, v5\ns_barrier\nv_mul_f32 v17, v19, v3\nv_mul_f32 v20, v23, v5\nv_mul_f32 v21, v23, v3\n" \
  "v_mul_f32 v24, v27, v2\nv_mul_f32 v25, v27, v0\nv_mul_f32 v34, v33, v8\nv_mul_f32 v35, v33, v6\nv_cvt_pk_f16_f32 v52, v18, v19\nv_cvt_pk_f16_f32 v53, v22, v23\n" \
  "v_cvt_pk_f16_f32 v54, v26, -v27\nv_cvt_pk_f16_f32 v55, v28, -v29\nv_fmac_f32 v16, v18, v3\nv_fmac_f32 v17, v18, v4\nv_fmac_f32 v20, v22, v3\nv_fmac_f32 v21, v22, v4\n" \
  "v_fmac_f32 v24, v26, v0\nv_fmac_f32 v25, v26, v1\nv_fmac_f32 v34, v32, v6\nv_fmac_f32 v35, v32, v7\nds_write2st64_b32 v69, v52, v53 offset0:37 offset1:53\n" \
  "ds_write2st64_b32 v69, v54, v55 offset0:5 offset1:21\nv_mul_f32 v18, v17, v5\nv_mul_f32 v19, v17, v3\nv_mul_f32 v22, v21, v5\nv_mul_f32 v23, v21, v3\n" \
  "v_mul_f32 v26, v25, v2\nv_mul_f32 v27, v25, v0\nv_mul_f32 v28, v35, v14\nv_mul_f32 v29, v35, v12\nv_cvt_pk_f16_f32 v52, v16, v17\nv_cvt_pk_f16_f32 v53, v20, v21\n" \
  "v_cvt_pk_f16_f32 v54, v24, -v25\nv_cvt_pk_f16_f32 v55, v34, -v35\nv_fmac_f32 v18, v16, v3\nv_fmac_f32 v19, v16, v4\nv_fmac_f32 v22, v20, v3\nv_fmac_f32 v23, v20, v4\n" \
  "v_fmac_f32 v26, v24, v0\nv_fmac_f32 v27, v24, v1\nv_fmac_f32 v28, v34, v12\nv_fmac_f32 v29, v34, v13\nds_write2st64_b32 v70, v52, v53 offset0:38 offset1:54\ns_barrier\n" \
  "ds_write2st64_b32 v70, v54, v55 offset0:6 offset1:22\nv_mul_f32 v16, v19, v5\nv_mul_f32 v17, v19, v3\nv_mul_f32 v20, v23, v5\nv_mul_f32 v21, v23, v3\n" \
  "v_mul_f32 v32, v35, v8\nv_mul_f32 v33, v35, v6\nv_cvt_pk_f16_f32 v52, v18, v19\nv_cvt_pk_f16_f32 v53, v22, v23\nv_cvt_pk_f16_f32 v54, v26, -v27\n" \
  "v_cvt_pk_f16_f32 v55, v28, -v29\nv_fmac_f32 v16, v18, v3\nv_fmac_f32 v17, v18, v4\nv_fmac_f32 v20, v22, v3\nv_fmac_f32 v21, v22, v4\nv_fmac_f32 v32, v34, v6\n" \
  "v_fmac_f32 v33, v34, v7\nds_write2st64_b32 v71, v52, v53 offset0:39 offset1:55\nds_write2st64_b32 v71, v54, v55 offset0:7 offset1:23\nv_mul_f32 v18, v17, v5\n" \
  "v_mul_f32 v19, v17, v3\nv_mul_f32 v22, v21, v5\nv_mul_f32 v23, v21, v3\nv_mul_f32 v34, v33, v8\nv_mul_f32 v35, v33, v6\nv_cvt_pk_f16_f32 v52, v16, v17\n" \
  "v_cvt_pk_f16_f32 v53, v20, v21\nv_cvt_pk_f16_f32 v56, v32, -v33\nv_fmac_f32 v18, v16, v3\nv_fmac_f32 v19, v16, v4\nv_fmac_f32 v22, v20, v3\nv_fmac_f32 v23, v20, v4\n" \
  "v_fmac_f32 v34, v32, v6\nv_fmac_f32 v35, v32, v7\nds_write2st64_b32 v72, v52, v53 offset0:40 offset1:56\ns_barrier\nv_mul_f32 v16, v19, v5\nv_mul_f32 v17, v19, v3\n" \
  "v_mul_f32 v20, v23, v5\nv_mul_f32 v21, v23, v3\nv_mul_f32 v32, v35, v8\nv_mul_f32 v33, v35, v6\nv_cvt_pk_f16_f32 v52, v18, v19\nv_cvt_pk_f16_f32 v53, v22, v23\n" \
  "v_cvt_pk_f16_f32 v57, v34, -v35\nv_fmac_f32 v16, v18, v3\nv_fmac_f32 v17, v18, v4\nv_fmac_f32 v20, v22, v3\nv_fmac_f32 v21, v22, v4\nv_fmac_f32 v32, v34, v6\n" \
  "v_fmac_f32 v33, v34, v7\nds_write2st64_b32 v73, v52, v53 offset0:41 offset1:57\nv_mul_f32 v18, v17, v5\nv_mul_f32 v19, v17, v3\nv_mul_f32 v22, v21, v5\n" \
  "v_mul_f32 v23, v21, v3\nv_mul_f32 v34, v33, v8\nv_mul_f32 v35, v33, v6\nv_cvt_pk_f16_f32 v52, v16, v17\nv_cvt_pk_f16_f32 v53, v20, v21\nv_cvt_pk_f16_f32 v54, v32, -v33\n" \
  "v_fmac_f32 v18, v16, v3\nv_fmac_f32 v19, v16, v4\nv_fmac_f32 v22, v20, v3\nv_fmac_f32 v23, v20, v4\nv_fmac_f32 v34, v32, v6\nv_fmac_f32 v35, v32, v7\n" \
  "ds_write2st64_b32 v74, v52, v53 offset0:42 offset1:58\nds_write_b32 v74, v54 offset:2560\nv_mul_f32 v16, v19, v5\nv_mul_f32 v17, v19, v3\nv_mul_f32 v20, v23, v5\n" \
  "v_mul_f32 v21, v23, v3\ns_barrier\nv_mul_f32 v32, v35, v8\nv_mul_f32 v33, v35, v6\nv_cvt_pk_f16_f32 v52, v18, v19\nv_cvt_pk_f16_f32 v53, v22, v23\n" \
  "v_cvt_pk_f16_f32 v55, v34, -v35\nv_fmac_f32 v16, v18, v3\nv_fmac_f32 v17, v18, v4\nv_fmac_f32 v20, v22, v3\nv_fmac_f32 v21, v22, v4\nv_fmac_f32 v32, v34, v6\n" \
  "v_fmac_f32 v33, v34, v7\nds_write2st64_b32 v75, v52, v53 offset0:43 offset1:59\nds_write2st64_b32 v72, v56, v55 offset0:8 offset1:24\nv_mul_f32 v18, v17, v5\n" \
  "v_mul_f32 v19, v17, v3\nv_mul_f32 v22, v21, v5\nv_mul_f32 v23, v21, v3\nv_cvt_pk_f16_f32 v52, v16, v17\nv_cvt_pk_f16_f32 v53, v20, v21\nv_cvt_pk_f16_f32 v55, v32, -v33\n" \
  "v_fmac_f32 v18, v16, v3\nv_fmac_f32 v19, v16, v4\nv_fmac_f32 v22, v20, v3\nv_fmac_f32 v23, v20, v4\nds_write2st64_b32 v76, v52, v53 offset0:44 offset1:60\n" \
  "ds_write2st64_b32 v73, v57, v55 offset0:9 offset1:25\nv_mul_f32 v16, v19, v5\nv_mul_f32 v17, v19, v3\nv_mul_f32 v20, v23, v5\nv_mul_f32 v21, v23, v3\n" \
  "v_cvt_pk_f16_f32 v52, v18, v19\nv_cvt_pk_f16_f32 v53, v22, v23\nv_fmac_f32 v16, v18, v3\nv_fmac_f32 v17, v18, v4\nv_fmac_f32 v20, v22, v3\ns_barrier\n" \
  "v_fmac_f32 v21, v22, v4\nds_write2st64_b32 v77, v52, v53 offset0:45 offset1:61\nv_mul_f32 v18, v17, v5\nv_mul_f32 v19, v17, v3\nv_mul_f32 v22, v21, v5\n" \
  "v_mul_f32 v23, v21, v3\nv_cvt_pk_f16_f32 v52, v16, v17\nv_cvt_pk_f16_f32 v53, v20, v21\nv_fmac_f32 v18, v16, v3\nv_fmac_f32 v19, v16, v4\nv_fmac_f32 v22, v20, v3\n" \
  "v_fmac_f32 v23, v20, v4\nds_write2st64_b32 v78, v52, v53 offset0:46 offset1:62\nv_cvt_pk_f16_f32 v52, v18, v19\nv_cvt_pk_f16_f32 v53, v22, v23\n" \
  "ds_write2st64_b32 v79, v52, v53 offset0:47 offset1:63\nds_read_b128 v[0:3], v80\nds_read_b128 v[4:7], v80 offset:8192\nds_read_b128 v[8:11], v81\n" \
  "ds_read_b128 v[12:15], v81 offset:8192\nds_read_b128 v[16:19], v82\nds_read_b128 v[20:23], v82 offset:8192\nds_read_b128 v[24:27], v83\n" \
  "ds_read_b128 v[28:31], v83 offset:8192\nds_read_b128 v[32:35], v84\nds_read_b128 v[36:39], v84 offset:8192\nds_read_b128 v[40:43], v85\n" \
  "ds_read_b128 v[44:47], v85 offset:8192\nds_read_b128 v[48:51], v86\nds_read_b128 v[52:55], v86 offset:8192\nds_read_b128 v[56:59], v87\n" \
  "ds_read_b128 v[60:63], v87 offset:8192\ns_setprio 2\ns_waitcnt lgkmcnt(14)\nv_mfma_f32_32x32x16_f16 %[acc], v[0:3], v[4:7], %[acc]\ns_barrier\ns_waitcnt lgkmcnt(12)\n" \
  "v_mfma_f32_32x32x16_f16 %[acc], v[8:11], v[12:15], %[acc]\ns_waitcnt lgkmcnt(10)\nv_mfma_f32_32x32x16_f16 %[acc], v[16:19], v[20:23], %[acc]\ns_waitcnt lgkmcnt(8)\n" \
  "v_mfma_f32_32x32x16_f16 %[acc], v[24:27], v[28:31], %[acc]\ns_waitcnt lgkmcnt(6)\nv_mfma_f32_32x32x16_f16 %[acc], v[32:35], v[36:39], %[acc]\ns_waitcnt lgkmcnt(4)\n" \
  "v_mfma_f32_32x32x16_f16 %[acc], v[40:43], v[44:47], %[acc]\ns_waitcnt lgkmcnt(2)\nv_mfma_f32_32x32x16_f16 %[acc], v[48:51], v[52:55], %[acc]\ns_waitcnt lgkmcnt(0)\n" \
  "v_mfma_f32_32x32x16_f16 %[acc], v[56:59], v[60:63], %[acc]\ns_setprio 0\ns_barrier\nv_mul_f32 v41, 0x41000000, %[ai1]\nv_mul_f32 v37, 0x41000000, %[a21]\n" \
  "v_mul_f32 v42, 0x43000000, %[ai1]\nv_mul_f32 v38, 0x43000000, %[a21]\nv_mul_f32 v43, 0x43800000, %[ai1]\nv_mul_f32 v39, 0x43800000, %[a21]\nv_fract_f32 v40, %[ai1]\n" \
  "v_fract_f32 v41, v41\nv_fract_f32 v42, v42\nv_fract_f32 v43, v43\nv_exp_f32 v36, %[a21]\nv_exp_f32 v37, v37\nv_exp_f32 v38, v38\nv_exp_f32 v39, v39\nv_cos_f32 v44, v40\n" \
  "v_sin_f32 v48, v40\nv_cos_f32 v45, v41\nv_sin_f32 v49, v41\nv_cos_f32 v46, v42\nv_sin_f32 v50, v42\nv_cos_f32 v47, v43\nv_sin_f32 v51, v43\nv_mov_b32 v52, 0x3c00\n" \
  "v_mul_f32 v0, v36, v44\nv_mul_f32 v1, v36, v48\nv_mul_f32 v2, v36, -v48\nv_mul_f32 v3, v37, v45\nv_mul_f32 v4, v37, v49\nv_mul_f32 v5, v37, -v49\n" \
  "v_mul_f32 v20, v38, v46\nv_mul_f32 v21, v38, v50\nv_mul_f32 v6, v39, v47\nv_mul_f32 v7, v39, v51\nv_mul_f32 v8, v39, -v51\nv_mul_f32 v9, v1, v2\nv_mul_f32 v10, v1, v0\n" \
  "v_mul_f32 v28, %[ci1], v8\nv_mul_f32 v29, %[ci1], v6\nv_mul_f32 v26, %[ci1], v2\nv_mul_f32 v27, %[ci1], v0\ns_barrier\nv_cvt_pk_f16_f32 v53, v20, v21\n" \
  "v_cvt_pk_f16_f32 v54, %[cr1], -%[ci1]\nv_fmac_f32 v9, v0, v0\nv_fmac_f32 v10, v0, v1\nv_fmac_f32 v28, %[cr1], v6\nv_fmac_f32 v29, %[cr1], v7\nv_fmac_f32 v26, %[cr1], v0\n" \
  "v_fmac_f32 v27, %[cr1], v1\nds_write2st64_b32 v64, v52, v53 offset0:32 offset1:48\nv_xor_b32 v11, 0x80000000, v10\nv_mul_f32 v22, v21, v5\nv_mul_f32 v23, v21, v3\n" \
  "v_mul_f32 v12, v10, v11\nv_mul_f32 v13, v10, v9\nv_mul_f32 v32, v29, v8\nv_mul_f32 v33, v29, v6\nv_mul_f32 v30, v29, v11\nv_mul_f32 v31, v29, v9\n" \
  "v_cvt_pk_f16_f32 v55, v28, -v29\nv_fmac_f32 v22, v20, v3\nv_fmac_f32 v23, v20, v4\nv_fmac_f32 v12, v9, v9\nv_fmac_f32 v13, v9, v10\nv_fmac_f32 v32, v28, v6\n" \
  "v_fmac_f32 v33, v28, v7\nv_fmac_f32 v30, v28, v9\nv_fmac_f32 v31, v28, v10\nds_write2st64_b32 v64, v54, v55 offset0:0 offset1:16\nv_xor_b32 v14, 0x80000000, v13\n" \
  "v_mul_f32 v16, v4, v5\nv_mul_f32 v17, v4, v3\nv_mul_f32 v20, v23, v5\nv_mul_f32 v21, v23, v3\nv_mul_f32 v24, v27, v2\nv_mul_f32 v25, v27, v0\nv_mul_f32 v28, v31, v11\n" \
  "v_mul_f32 v29, v31, v9\nv_cvt_pk_f16_f32 v52, v3, v4\nv_cvt_pk_f16_f32 v53, v22, v23\nv_cvt_pk_f16_f32 v54, v26, -v27\nv_cvt_pk_f16_f32 v55, v30, -v31\n" \
  "v_fmac_f32 v16, v3, v3\nv_fmac_f32 v17, v3, v4\nv_fmac_f32 v20, v22, v3\nv_fmac_f32 v21, v22, v4\nv_fmac_f32 v24, v26, v0\nv_fmac_f32 v25, v26, v1\n" \
  "v_fmac_f32 v28, v30, v9\nv_fmac_f32 v29, v30, v10\nds_write2st64_b32 v65, v52, v53 offset0:33 offset1:49\nds_write2st64_b32 v65, v54, v55 offset0:1 offset1:17\n" \
  "v_mul_f32 v18, v17, v5\nv_mul_f32 v19, v17, v3\nv_mul_f32 v22, v21, v5\nv_mul_f32 v23, v21, v3\nv_mul_f32 v26, v25, v2\nv_mul_f32 v27, v25, v0\nv_mul_f32 v30, v29, v11\n" \
  "v_mul_f32 v31, v29, v9\nv_cvt_pk_f16_f32 v52, v16, v17\nv_cvt_pk_f16_f32 v53, v20, v21\nv_cvt_pk_f16_f32 v54, v24, -v25\nv_cvt_pk_f16_f32 v55, v28, -v29\n" \
  "v_fmac_f32 v18, v16, v3\nv_fmac_f32 v19, v16, v4\nv_fmac_f32 v22, v20, v3\nv_fmac_f32 v23, v20, v4\nv_fmac_f32 v26, v24, v0\nv_fmac_f32 v27, v24, v1\n" \
  "v_fmac_f32 v30, v28, v9\nv_fmac_f32 v31, v28, v10\nds_write2st64_b32 v66, v52, v53 offset0:34 offset1:50\nds_write2st64_b32 v66, v54, v55 offset0:2 offset1:18\n" \
  "v_mul_f32 v16, v19, v5\nv_mul_f32 v17, v19, v3\nv_mul_f32 v20, v23, v5\nv_mul_f32 v21, v23, v3\nv_mul_f32 v24, v27, v2\nv_mul_f32 v25, v27, v0\n" \
  "v_cvt_pk_f16_f32 v52, v18, v19\nv_cvt_pk_f16_f32 v53, v22, v23\nv_cvt_pk_f16_f32 v54, v26, -v27\nv_cvt_pk_f16_f32 v55, v30, -v31\nv_fmac_f32 v16, v18, v3\n" \
  "v_fmac_f32 v17, v18, v4\nv_fmac_f32 v20, v22, v3\nv_fmac_f32 v21, v22, v4\nv_fmac_f32 v24, v26, v0\nv_fmac_f32 v25, v26, v1\n" \
  "ds_write2st64_b32 v67, v52, v53 offset0:35 offset1:51\nds_write2st64_b32 v67, v54, v55 offset0:3 offset1:19\nv_mul_f32 v18, v17, v5\nv_mul_f32 v19, v17, v3\n" \
  "v_mul_f32 v22, v21, v5\nv_mul_f32 v23, v21, v3\nv_mul_f32 v26, v25, v2\nv_mul_f32 v27, v25, v0\nv_mul_f32 v28, v33, v14\nv_mul_f32 v29, v33, v12\n" \
  "v_cvt_pk_f16_f32 v52, v16, v17\nv_cvt_pk_f16_f32 v53, v20, v21\nv_cvt_pk_f16_f32 v54, v24, -v25\nv_cvt_pk_f16_f32 v55, v32, -v33\nv_fmac_f32 v18, v16, v3\n" \
  "v_fmac_f32 v19, v16, v4\nv_fmac_f32 v22, v20, v3\nv_fmac_f32 v23, v20, v4\nv_fmac_f32 v26, v24, v0\nv_fmac_f32 v27, v24, v1\nv_fmac_f32 v28, v32, v12\n" \
  "v_fmac_f32 v29, v32, v13\nds_write2st64_b32 v68, v52, v53 offset0:36 offset1:52\nds_write2st64_b32 v68, v54, v55 offset0:4 offset1:20\nv_mul_f32 v16, v19, v5\n" \
  "v_mul_f32 v17, v19, v3\nv_mul_f32 v20, v23, v5\nv_mul_f32 v21, v23, v3\nv_mul_f32 v24, v27, v2\nv_mul_f32 v25, v27, v0\nv_mul_f32 v34, v33, v8\nv_mul_f32 v35, v33, v6\n" \
  "v_cvt_pk_f16_f32 v52, v18, v19\nv_cvt_pk_f16_f32 v53, v22, v23\nv_cvt_pk_f16_f32 v54, v26, -v27\nv_cvt_pk_f16_f32 v55, v28, -v29\nv_fmac_f32 v16, v18, v3\n" \
  "v_fmac_f32 v17, v18, v4\nv_fmac_f32 v20, v22, v3\nv_fmac_f32 v21, v22, v4\nv_fmac_f32 v24, v26, v0\nv_fmac_f32 v25, v26, v1\nv_fmac_f32 v34, v32, v6\n" \
  "v_fmac_f32 v35, v32, v7\nds_write2st64_b32 v69, v52, v53 offset0:37 offset1:53\nds_write2st64_b32 v69, v54, v55 offset0:5 offset1:21\nv_mul_f32 v18, v17, v5\n" \
  "v_mul_f32 v19, v17, v3\nv_mul_f32 v22, v21, v5\nv_mul_f32 v23, v21, v3\nv_mul_f32 v26, v25, v2\nv_mul_f32 v27, v25, v0\nv_mul_f32 v28, v35, v14\nv_mul_f32 v29, v35, v12\n" \
  "v_cvt_pk_f16_f32 v52, v16, v17\nv_cvt_pk_f16_f32 v53, v20, v21\nv_cvt_pk_f16_f32 v54, v24, -v25\nv_cvt_pk_f16_f32 v55, v34, -v35\nv_fmac_f32 v18, v16, v3\n" \
  "v_fmac_f32 v19, v16, v4\nv_fmac_f32 v22, v20, v3\nv_fmac_f32 v23, v20, v4\nv_fmac_f32 v26, v24, v0\nv_fmac_f32 v27, v24, v1\nv_fmac_f32 v28, v34, v12\n" \
  "v_fmac_f32 v29, v34, v13\nds_write2st64_b32 v70, v52, v53 offset0:38 offset1:54\nds_write2st64_b32 v70, v54, v55 offset0:6 offset1:22\nv_mul_f32 v16, v19, v5\n" \
  "v_mul_f32 v17, v19, v3\nv_mul_f32 v20, v23, v5\nv_mul_f32 v21, v23, v3\nv_mul_f32 v32, v35, v8\nv_mul_f32 v33, v35, v6\nv_cvt_pk_f16_f32 v52, v18, v19\n" \
  "v_cvt_pk_f16_f32 v53, v22, v23\nv_cvt_pk_f16_f32 v54, v26, -v27\nv_cvt_pk_f16_f32 v55, v28, -v29\nv_fmac_f32 v16, v18, v3\nv_fmac_f32 v17, v18, v4\n" \
  "v_fmac_f32 v20, v22, v3\nv_fmac_f32 v21, v22, v4\nv_fmac_f32 v32, v34, v6\nv_fmac_f32 v33, v34, v7\nds_write2st64_b32 v71, v52, v53 offset0:39 offset1:55\n" \
  "ds_write2st64_b32 v71, v54, v55 offset0:7 offset1:23\nv_mul_f32 v18, v17, v5\nv_mul_f32 v19, v17, v3\nv_mul_f32 v22, v21, v5\nv_mul_f32 v23, v21, v3\n" \
  "v_mul_f32 v34, v33, v8\nv_mul_f32 v35, v33, v6\nv_cvt_pk_f16_f32 v52, v16, v17\nv_cvt_pk_f16_f32 v53, v20, v21\nv_cvt_pk_f16_f32 v56, v32, -v33\nv_fmac_f32 v18, v16, v3\n" \
  "v_fmac_f32 v19, v16, v4\nv_fmac_f32 v22, v20, v3\nv_fmac_f32 v23, v20, v4\nv_fmac_f32 v34, v32, v6\nv_fmac_f32 v35, v32, v7\n" \
  "ds_write2st64_b32 v72, v52, v53 offset0:40 offset1:56\nv_mul_f32 v16, v19, v5\nv_mul_f32 v17, v19, v3\nv_mul_f32 v20, v23, v5\nv_mul_f32 v21, v23, v3\n" \
  "v_mul_f32 v32, v35, v8\nv_mul_f32 v33, v35, v6\nv_cvt_pk_f16_f32 v52, v18, v19\nv_cvt_pk_f16_f32 v53, v22, v23\nv_cvt_pk_f16_f32 v57, v34, -v35\nv_fmac_f32 v16, v18, v3\n" \
  "v_fmac_f32 v17, v18, v4\nv_fmac_f32 v20, v22, v3\nv_fmac_f32 v21, v22, v4\nv_fmac_f32 v32, v34, v6\nv_fmac_f32 v33, v34, v7\n" \
  "ds_write2st64_b32 v73, v52, v53 offset0:41 offset1:57\nv_mul_f32 v18, v17, v5\nv_mul_f32 v19, v17, v3\nv_mul_f32 v22, v21, v5\nv_mul_f32 v23, v21, v3\n" \
  "v_mul_f32 v34, v33, v8\nv_mul_f32 v35, v33, v6\nv_cvt_pk_f16_f32 v52, v16, v17\nv_cvt_pk_f16_f32 v53, v20, v21\nv_cvt_pk_f16_f32 v54, v32, -v33\nv_fmac_f32 v18, v16, v3\n" \
  "v_fmac_f32 v19, v16, v4\nv_fmac_f32 v22, v20, v3\nv_fmac_f32 v23, v20, v4\nv_fmac_f32 v34, v32, v6\nv_fmac_f32 v35, v32, v7\n" \
  "ds_write2st64_b32 v74, v52, v53 offset0:42 offset1:58\nds_write_b32 v74, v54 offset:2560\nv_mul_f32 v16, v19, v5\nv_mul_f32 v17, v19, v3\nv_mul_f32 v20, v23, v5\n" \
  "v_mul_f32 v21, v23, v3\nv_mul_f32 v32, v35, v8\nv_mul_f32 v33, v35, v6\nv_cvt_pk_f16_f32 v52, v18, v19\nv_cvt_pk_f16_f32 v53, v22, v23\nv_cvt_pk_f16_f32 v55, v34, -v35\n" \
  "v_fmac_f32 v16, v18, v3\nv_fmac_f32 v17, v18, v4\nv_fmac_f32 v20, v22, v3\nv_fmac_f32 v21, v22, v4\nv_fmac_f32 v32, v34, v6\nv_fmac_f32 v33, v34, v7\n" \
  "ds_write2st64_b32 v75, v52, v53 offset0:43 offset1:59\nds_write2st64_b32 v72, v56, v55 offset0:8 offset1:24\nv_mul_f32 v18, v17, v5\nv_mul_f32 v19, v17, v3\n" \
  "v_mul_f32 v22, v21, v5\nv_mul_f32 v23, v21, v3\nv_cvt_pk_f16_f32 v52, v16, v17\nv_cvt_pk_f16_f32 v53, v20, v21\nv_cvt_pk_f16_f32 v55, v32, -v33\nv_fmac_f32 v18, v16, v3\n" \
  "v_fmac_f32 v19, v16, v4\nv_fmac_f32 v22, v20, v3\nv_fmac_f32 v23, v20, v4\nds_write2st64_b32 v76, v52, v53 offset0:44 offset1:60\n" \
  "ds_write2st64_b32 v73, v57, v55 offset0:9 offset1:25\nv_mul_f32 v16, v19, v5\nv_mul_f32 v17, v19, v3\nv_mul_f32 v20, v23, v5\nv_mul_f32 v21, v23, v3\n" \
  "v_cvt_pk_f16_f32 v52, v18, v19\nv_cvt_pk_f16_f32 v53, v22, v23\nv_fmac_f32 v16, v18, v3\nv_fmac_f32 v17, v18, v4\nv_fmac_f32 v20, v22, v3\nv_fmac_f32 v21, v22, v4\n" \
  "ds_write2st64_b32 v77, v52, v53 offset0:45 offset1:61\nv_mul_f32 v18, v17, v5\nv_mul_f32 v19, v17, v3\nv_mul_f32 v22, v21, v5\nv_mul_f32 v23, v21, v3\n" \
  "v_cvt_pk_f16_f32 v52, v16, v17\nv_cvt_pk_f16_f32 v53, v20, v21\nv_fmac_f32 v18, v16, v3\nv_fmac_f32 v19, v16, v4\nv_fmac_f32 v22, v20, v3\nv_fmac_f32 v23, v20, v4\n" \
  "ds_write2st64_b32 v78, v52, v53 offset0:46 offset1:62\nv_cvt_pk_f16_f32 v52, v18, v19\nv_cvt_pk_f16_f32 v53, v22, v23\n" \
  "ds_write2st64_b32 v79, v52, v53 offset0:47 offset1:63\nds_read_b128 v[0:3], v80\nds_read_b128 v[4:7], v80 offset:8192\nds_read_b128 v[8:11], v81\n" \
  "ds_read_b128 v[12:15], v81 offset:8192\nds_read_b128 v[16:19], v82\nds_read_b128 v[20:23], v82 offset:8192\nds_read_b128 v[24:27], v83\n" \
  "ds_read_b128 v[28:31], v83 offset:8192\nds_read_b128 v[32:35], v84\nds_read_b128 v[36:39], v84 offset:8192\nds_read_b128 v[40:43], v85\n" \
  "ds_read_b128 v[44:47], v85 offset:8192\nds_read_b128 v[48:51], v86\nds_read_b128 v[52:55], v86 offset:8192\nds_read_b128 v[56:59], v87\n" \
  "ds_read_b128 v[60:63], v87 offset:8192\ns_setprio 2\ns_waitcnt lgkmcnt(14)\nv_mfma_f32_32x32x16_f16 %[acc], v[0:3], v[4:7], %[acc]\ns_waitcnt lgkmcnt(12)\n" \
  "v_mfma_f32_32x32x16_f16 %[acc], v[8:11], v[12:15], %[acc]\ns_waitcnt lgkmcnt(10)\nv_mfma_f32_32x32x16_f16 %[acc], v[16:19], v[20:23], %[acc]\ns_waitcnt lgkmcnt(8)\n" \
  "v_mfma_f32_32x32x16_f16 %[acc], v[24:27], v[28:31], %[acc]\ns_waitcnt lgkmcnt(6)\nv_mfma_f32_32x32x16_f16 %[acc], v[32:35], v[36:39], %[acc]\ns_waitcnt lgkmcnt(4)\n" \
  "v_mfma_f32_32x32x16_f16 %[acc], v[40:43], v[44:47], %[acc]\ns_waitcnt lgkmcnt(2)\nv_mfma_f32_32x32x16_f16 %[acc], v[48:51], v[52:55], %[acc]\ns_waitcnt lgkmcnt(0)\n" \
  "v_mfma_f32_32x32x16_f16 %[acc], v[56:59], v[60:63], %[acc]\ns_setprio 0\ns_nop 11\n"
#define CLOB64 "v0", "v1", "v2", "v3", "v4", "v5", "v6", "v7", "v8", "v9", "v10", "v11", "v12", "v13", "v14", "v15", "v16", "v17", "v18", "v19", "v20", "v21", \
  "v22", "v23", "v24", "v25", "v26", "v27", "v28", "v29", "v30", "v31", "v32", "v33", "v34", "v35", "v36", "v37", "v38", "v39", "v40", "v41", "v42", "v43", \
  "v44", "v45", "v46", "v47", "v48", "v49", "v50", "v51", "v52", "v53", "v54", "v55", "v56", "v57", "v58", "v59", "v60", "v61", "v62", "v63"
DI void kgen(const P& p, int c, int lane, Pre& pre, u32x2& tag) {
  __builtin_amdgcn_s_setprio(KG_PRIO);
  const unsigned scr = L_W4 + c * W4_BYTES, kc = L_KC + c * 2048;
  const int r = lane & 31, h = lane >> 5;
  u32x16 aw; u32x8 ar;
#pragma unroll
  for (int i = 0; i < 16; i++) aw[i] = scr + ((lane * 4) ^ (i << 4));
#pragma unroll
  for (int k = 0; k < 8; k++) ar[k] = scr + r * 256 + (((2 * k + h) ^ (r & 15)) << 4);
  asm volatile("" : "+{v[64:79]}"(aw), "+{v[80:87]}"(ar));
  precompute(pre);
  STAMP(20); bar(); STAMP(21);
  const unsigned pa = L_PAR + c * 256 + lane * 4;
  const unsigned rl = ldsr<unsigned>(pa), ri = ldsr<unsigned>(pa + 768), rc = ldsr<unsigned>(pa + 1536), rd = ldsr<unsigned>(pa + 2304);
  const unsigned short ldt = ldsr<unsigned short>(L_PAR + 3072 + c * 4);
  const float dt = rbf(expf(bf2f(ldt)));
  auto lo = [](unsigned v) { return __uint_as_float(v << 16); };
  auto hi = [](unsigned v) { return __uint_as_float(v & 0xffff0000u); };
  float a2[2] = {-rbf(expf(lo(rl))) * dt * 1.44269504f, -rbf(expf(hi(rl))) * dt * 1.44269504f};
  float ai[2] = {lo(ri) * dt * 0.15915494f, hi(ri) * dt * 0.15915494f};
  float cr[2] = {lo(rc) * dt * 2.f, hi(rc) * dt * 2.f}, ci[2] = {lo(rd) * dt * 2.f, hi(rd) * dt * 2.f};
  const float mc = __uint_as_float(wave_max_u32(__float_as_uint(fmaxf(fmaxf(fabsf(cr[0]), fabsf(ci[0])), fmaxf(fabsf(cr[1]), fabsf(ci[1]))))));
  const int e = (mc > 0.f && mc < 3e38f) ? 8 - __builtin_amdgcn_frexp_expf(mc) : 0;
  if (lane == 0) { ldsw<int>(L_MISC + 16 + c * 4, e); }
#pragma unroll
  for (int s = 0; s < 2; s++) { cr[s] = ldexpf(cr[s], e); ci[s] = ldexpf(ci[s], e); }
  f32x16 acc;
#pragma unroll
  for (int i = 0; i < 16; i++) acc[i] = 0.f;
  tag = call_tag();
  asm volatile("" : "+v"(tag));
  STAMP(22);
  asm volatile(KGEN_ASM : [acc] "+v"(acc) : [a20] "v"(a2[0]), [ai0] "v"(ai[0]), [cr0] "v"(cr[0]), [ci0] "v"(ci[0]), [a21] "v"(a2[1]), [ai1] "v"(ai[1]), [cr1] "v"(cr[1]), [ci1] "v"(ci[1]),
               "{v[64:79]}"(aw), "{v[80:87]}"(ar) : "memory", CLOB64);
  STAMP(30);
  ldsw<u32x2>(kc + (8 * r + 4 * h) * 2, u32x2{pk_f16(acc[0], acc[1]), pk_f16(acc[2], acc[3])});
  ldsw<unsigned>(kc + (h ? 384 + 2 * r : 256 + 4 * r) * 2, pk_f16(acc[8], acc[9]));
  ldsw<unsigned>(kc + (h ? 448 + 2 * r : 258 + 4 * r) * 2, pk_f16(acc[10], acc[11]));
  if (!h) {
#pragma unroll
    for (int i = 0; i < 3; i++) ldsw<_Float16>(kc + (512 + 32 * i + r) * 2, (_Float16)acc[4 + i]);
    ldsw<_Float16>(kc + (608 + r) * 2, (_Float16)acc[12]); ldsw<_Float16>(kc + (640 + r) * 2, (_Float16)acc[13]);
  } else ldsw<unsigned short>(kc + 672 * 2, 0);
  if (lane < 8) ldsw<u32x4>(scr + lane * 16, u32x4{0, 0, 0, 0});
  STAMP(24);
  __builtin_amdgcn_s_setprio(0);
}
DI void windows(const Pre& pre) {
  unsigned k[3][7];
#pragma unroll
  for (int c = 0; c < 3; c++)
#pragma unroll
    for (int i = 0; i < 7; i++) k[c][i] = ldsr<unsigned short>(pre.wa[i] + c * 2048);
#pragma unroll
  for (int c = 0; c < 3; c++) {
    auto D = [&](int y) { return k[c][y + 3] | (k[c][y + 2] << 16); };
    ldsw<u32x4>(pre.ww + c * W4_BYTES, u32x4{D(0), D(-2), D(1), D(-1)});
    ldsw<u32x4>(pre.ww + c * W4_BYTES + 16, u32x4{D(2), D(0), D(3), D(1)});
  }
}

#define CONV_ASM0 \
  "ds_read_b64 v[0:1], %[aA] offset:32\nds_read_b64 v[2:3], %[aA] offset:0\nds_read_b128 v[60:63], %[aB] offset:3712\nds_read_b128 v[48:51], %[aB] offset:2176\n" \
  "ds_read_b128 v[44:47], %[aB] offset:1664\nds_read_b128 v[32:35], %[aB] offset:128\nds_read_b64 v[4:5], %[aA] offset:2080\nds_read_b64 v[6:7], %[aA] offset:2048\n" \
  "ds_read_b128 v[56:59], %[aB] offset:3200\nds_read_b128 v[40:43], %[aB] offset:1152\nds_read_b64 v[8:9], %[aA] offset:4128\nds_read_b64 v[10:11], %[aA] offset:4096\n" \
  "ds_read_b128 v[52:55], %[aB] offset:2688\nds_read_b128 v[36:39], %[aB] offset:640\nds_read_b64 v[12:13], %[aA] offset:6176\nds_read_b64 v[14:15], %[aA] offset:6144\n" \
  "ds_read_b64 v[16:17], %[aA] offset:8224\nds_read_b64 v[18:19], %[aA] offset:8192\nds_read_b64 v[20:21], %[aA] offset:10272\nds_read_b64 v[22:23], %[aA] offset:10240\n" \
  "ds_read_b64 v[24:25], %[aA] offset:12320\nds_read_b64 v[26:27], %[aA] offset:12288\nds_read_b64 v[28:29], %[aA] offset:14368\nds_read_b64 v[30:31], %[aA] offset:14336\n" \
  "1:\ns_waitcnt lgkmcnt(15)\nv_mfma_f32_16x16x32_f16 %[c3], v[0:3], v[60:63], %[c3]\nds_read_b64 v[64:65], %[aA] offset:288\nds_read_b64 v[66:67], %[aA] offset:256\n" \
  "s_waitcnt lgkmcnt(15)\nv_mfma_f32_16x16x32_f16 %[c2], v[0:3], v[48:51], %[c2]\nds_read_b128 v[124:127], %[aB] offset:3648\nds_read_b128 v[112:115], %[aB] offset:2112\n" \
  "s_waitcnt lgkmcnt(15)\nv_mfma_f32_16x16x32_f16 %[c1], v[0:3], v[44:47], %[c1]\nds_read_b128 v[108:111], %[aB] offset:1600\nds_read_b128 v[96:99], %[aB] offset:64\n" \
  "s_waitcnt lgkmcnt(15)\nv_mfma_f32_16x16x32_f16 %[c0], v[0:3], v[32:35], %[c0]\nds_read_b64 v[68:69], %[aA] offset:2336\nds_read_b64 v[70:71], %[aA] offset:2304\n" \
  "s_waitcnt lgkmcnt(15)\nv_mfma_f32_16x16x32_f16 %[c3], v[4:7], v[56:59], %[c3]\nds_read_b128 v[120:123], %[aB] offset:3136\nds_read_b128 v[104:107], %[aB] offset:1088\n" \
  "v_mfma_f32_16x16x32_f16 %[c2], v[4:7], v[44:47], %[c2]\nds_read_b64 v[72:73], %[aA] offset:4384\nds_read_b64 v[74:75], %[aA] offset:4352\ns_waitcnt lgkmcnt(15)\n" \
  "v_mfma_f32_16x16x32_f16 %[c1], v[4:7], v[40:43], %[c1]\nds_read_b128 v[116:119], %[aB] offset:2624\nds_read_b128 v[100:103], %[aB] offset:576\ns_waitcnt lgkmcnt(15)\n" \
  "v_mfma_f32_16x16x32_f16 %[c3], v[8:11], v[52:55], %[c3]\nds_read_b64 v[76:77], %[aA] offset:6432\nv_mfma_f32_16x16x32_f16 %[c2], v[8:11], v[40:43], %[c2]\n" \
  "ds_read_b64 v[78:79], %[aA] offset:6400\ns_waitcnt lgkmcnt(15)\nv_mfma_f32_16x16x32_f16 %[c1], v[8:11], v[36:39], %[c1]\nds_read_b64 v[80:81], %[aA] offset:8480\n" \
  "s_waitcnt lgkmcnt(15)\nv_mfma_f32_16x16x32_f16 %[c3], v[12:15], v[48:51], %[c3]\nds_read_b64 v[82:83], %[aA] offset:8448\n" \
  "v_mfma_f32_16x16x32_f16 %[c2], v[12:15], v[36:39], %[c2]\nds_read_b64 v[84:85], %[aA] offset:10528\nv_mfma_f32_16x16x32_f16 %[c1], v[12:15], v[32:35], %[c1]\n" \
  "ds_read_b64 v[86:87], %[aA] offset:10496\ns_waitcnt lgkmcnt(15)\nv_mfma_f32_16x16x32_f16 %[c3], v[16:19], v[44:47], %[c3]\nds_read_b64 v[88:89], %[aA] offset:12576\n" \
  "v_mfma_f32_16x16x32_f16 %[c2], v[16:19], v[32:35], %[c2]\nds_read_b64 v[90:91], %[aA] offset:12544\ns_waitcnt lgkmcnt(15)\n" \
  "v_mfma_f32_16x16x32_f16 %[c3], v[20:23], v[40:43], %[c3]\nds_read_b64 v[92:93], %[aA] offset:14624\ns_waitcnt lgkmcnt(15)\n" \
  "v_mfma_f32_16x16x32_f16 %[c3], v[24:27], v[36:39], %[c3]\nds_read_b64 v[94:95], %[aA] offset:14592\ns_waitcnt lgkmcnt(15)\n" \
  "v_mfma_f32_16x16x32_f16 %[c3], v[28:31], v[32:35], %[c3]\ns_sub_u32 %[n], %[n], 1\ns_cmp_eq_u32 %[n], 0\ns_cbranch_scc1 2f\ns_waitcnt lgkmcnt(15)\n" \
  "v_mfma_f32_16x16x32_f16 %[c3], v[64:67], v[124:127], %[c3]\nds_read_b64 v[0:1], %[aA] offset:544\nds_read_b64 v[2:3], %[aA] offset:512\ns_waitcnt lgkmcnt(15)\n" \
  "v_mfma_f32_16x16x32_f16 %[c2], v[64:67], v[112:115], %[c2]\nds_read_b128 v[60:63], %[aB] offset:3584\nds_read_b128 v[48:51], %[aB] offset:2048\ns_waitcnt lgkmcnt(15)\n" \
  "v_mfma_f32_16x16x32_f16 %[c1], v[64:67], v[108:111], %[c1]\nds_read_b128 v[44:47], %[aB] offset:1536\nds_read_b128 v[32:35], %[aB] offset:0\ns_waitcnt lgkmcnt(15)\n" \
  "v_mfma_f32_16x16x32_f16 %[c0], v[64:67], v[96:99], %[c0]\nds_read_b64 v[4:5], %[aA] offset:2592\nds_read_b64 v[6:7], %[aA] offset:2560\ns_waitcnt lgkmcnt(15)\n" \
  "v_mfma_f32_16x16x32_f16 %[c3], v[68:71], v[120:123], %[c3]\nds_read_b128 v[56:59], %[aB] offset:3072\nds_read_b128 v[40:43], %[aB] offset:1024\n" \
  "v_mfma_f32_16x16x32_f16 %[c2], v[68:71], v[108:111], %[c2]\nds_read_b64 v[8:9], %[aA] offset:4640\nds_read_b64 v[10:11], %[aA] offset:4608\ns_waitcnt lgkmcnt(15)\n" \
  "v_mfma_f32_16x16x32_f16 %[c1], v[68:71], v[104:107], %[c1]\nds_read_b128 v[52:55], %[aB] offset:2560\nds_read_b128 v[36:39], %[aB] offset:512\ns_waitcnt lgkmcnt(15)\n" \
  "v_mfma_f32_16x16x32_f16 %[c3], v[72:75], v[116:119], %[c3]\nds_read_b64 v[12:13], %[aA] offset:6688\nv_mfma_f32_16x16x32_f16 %[c2], v[72:75], v[104:107], %[c2]\n" \
  "ds_read_b64 v[14:15], %[aA] offset:6656\ns_waitcnt lgkmcnt(15)\nv_mfma_f32_16x16x32_f16 %[c1], v[72:75], v[100:103], %[c1]\nds_read_b64 v[16:17], %[aA] offset:8736\n" \
  "s_waitcnt lgkmcnt(15)\nv_mfma_f32_16x16x32_f16 %[c3], v[76:79], v[112:115], %[c3]\nds_read_b64 v[18:19], %[aA] offset:8704\n" \
  "v_mfma_f32_16x16x32_f16 %[c2], v[76:79], v[100:103], %[c2]\nds_read_b64 v[20:21], %[aA] offset:10784\nv_mfma_f32_16x16x32_f16 %[c1], v[76:79], v[96:99], %[c1]\n" \
  "ds_read_b64 v[22:23], %[aA] offset:10752\ns_waitcnt lgkmcnt(15)\nv_mfma_f32_16x16x32_f16 %[c3], v[80:83], v[108:111], %[c3]\nds_read_b64 v[24:25], %[aA] offset:12832\n" \
  "v_mfma_f32_16x16x32_f16 %[c2], v[80:83], v[96:99], %[c2]\nds_read_b64 v[26:27], %[aA] offset:12800\ns_waitcnt lgkmcnt(15)\n" \
  "v_mfma_f32_16x16x32_f16 %[c3], v[84:87], v[104:107], %[c3]\nds_read_b64 v[28:29], %[aA] offset:14880\ns_waitcnt lgkmcnt(15)\n" \
  "v_mfma_f32_16x16x32_f16 %[c3], v[88:91], v[100:103], %[c3]\nds_read_b64 v[30:31], %[aA] offset:14848\ns_waitcnt lgkmcnt(15)\n" \
  "v_mfma_f32_16x16x32_f16 %[c3], v[92:95], v[96:99], %[c3]\nv_add_u32 %[aA], 0x200, %[aA]\nv_subrev_u32 %[aB], 0x80, %[aB]\ns_branch 1b\n2:\n" \
  "buffer_load_dwordx4 %[pf], %[pa], %[ws], 0 offen sc1\ns_waitcnt lgkmcnt(15)\nv_mfma_f32_16x16x32_f16 %[c3], v[64:67], v[124:127], %[c3]\ns_waitcnt lgkmcnt(15)\n" \
  "v_mfma_f32_16x16x32_f16 %[c2], v[64:67], v[112:115], %[c2]\ns_waitcnt lgkmcnt(15)\nv_mfma_f32_16x16x32_f16 %[c1], v[64:67], v[108:111], %[c1]\ns_waitcnt lgkmcnt(15)\n" \
  "v_mfma_f32_16x16x32_f16 %[c0], v[64:67], v[96:99], %[c0]\ns_waitcnt lgkmcnt(15)\nv_mfma_f32_16x16x32_f16 %[c3], v[68:71], v[120:123], %[c3]\n" \
  "v_mfma_f32_16x16x32_f16 %[c2], v[68:71], v[108:111], %[c2]\ns_waitcnt lgkmcnt(14)\nv_mfma_f32_16x16x32_f16 %[c1], v[68:71], v[104:107], %[c1]\ns_waitcnt lgkmcnt(11)\n" \
  "v_mfma_f32_16x16x32_f16 %[c3], v[72:75], v[116:119], %[c3]\nv_mfma_f32_16x16x32_f16 %[c2], v[72:75], v[104:107], %[c2]\ns_waitcnt lgkmcnt(10)\n" \
  "v_mfma_f32_16x16x32_f16 %[c1], v[72:75], v[100:103], %[c1]\ns_waitcnt lgkmcnt(8)\nv_mfma_f32_16x16x32_f16 %[c3], v[76:79], v[112:115], %[c3]\n" \
  "v_mfma_f32_16x16x32_f16 %[c2], v[76:79], v[100:103], %[c2]\nv_mfma_f32_16x16x32_f16 %[c1], v[76:79], v[96:99], %[c1]\ns_waitcnt lgkmcnt(6)\n" \
  "v_mfma_f32_16x16x32_f16 %[c3], v[80:83], v[108:111], %[c3]\nv_mfma_f32_16x16x32_f16 %[c2], v[80:83], v[96:99], %[c2]\ns_waitcnt lgkmcnt(4)\n" \
  "v_mfma_f32_16x16x32_f16 %[c3], v[84:87], v[104:107], %[c3]\ns_waitcnt lgkmcnt(2)\nv_mfma_f32_16x16x32_f16 %[c3], v[88:91], v[100:103], %[c3]\ns_waitcnt lgkmcnt(0)\n" \
  "v_mfma_f32_16x16x32_f16 %[c3], v[92:95], v[96:99], %[c3]\ns_waitcnt vmcnt(0)\ns_nop 7\n"
#define CONV_ASM1 \
  "ds_read_b64 v[0:1], %[aA] offset:32\nds_read_b64 v[2:3], %[aA] offset:0\nds_read_b128 v[56:59], %[aB] offset:3200\nds_read_b128 v[52:55], %[aB] offset:2688\n" \
  "ds_read_b128 v[40:43], %[aB] offset:1152\nds_read_b128 v[36:39], %[aB] offset:640\nds_read_b64 v[4:5], %[aA] offset:2080\nds_read_b64 v[6:7], %[aA] offset:2048\n" \
  "ds_read_b128 v[48:51], %[aB] offset:2176\nds_read_b128 v[32:35], %[aB] offset:128\nds_read_b64 v[8:9], %[aA] offset:4128\nds_read_b64 v[10:11], %[aA] offset:4096\n" \
  "ds_read_b128 v[44:47], %[aB] offset:1664\nds_read_b64 v[12:13], %[aA] offset:6176\nds_read_b64 v[14:15], %[aA] offset:6144\nds_read_b64 v[16:17], %[aA] offset:8224\n" \
  "ds_read_b64 v[18:19], %[aA] offset:8192\nds_read_b64 v[20:21], %[aA] offset:10272\nds_read_b64 v[22:23], %[aA] offset:10240\nds_read_b64 v[24:25], %[aA] offset:12320\n" \
  "ds_read_b64 v[26:27], %[aA] offset:12288\n1:\ns_waitcnt lgkmcnt(15)\nv_mfma_f32_16x16x32_f16 %[c3], v[0:3], v[56:59], %[c3]\nds_read_b64 v[64:65], %[aA] offset:288\n" \
  "ds_read_b64 v[66:67], %[aA] offset:256\ns_waitcnt lgkmcnt(15)\nv_mfma_f32_16x16x32_f16 %[c2], v[0:3], v[52:55], %[c2]\nds_read_b128 v[120:123], %[aB] offset:3136\n" \
  "ds_read_b128 v[116:119], %[aB] offset:2624\ns_waitcnt lgkmcnt(15)\nv_mfma_f32_16x16x32_f16 %[c1], v[0:3], v[40:43], %[c1]\nds_read_b128 v[104:107], %[aB] offset:1088\n" \
  "ds_read_b128 v[100:103], %[aB] offset:576\ns_waitcnt lgkmcnt(15)\nv_mfma_f32_16x16x32_f16 %[c0], v[0:3], v[36:39], %[c0]\nds_read_b64 v[68:69], %[aA] offset:2336\n" \
  "ds_read_b64 v[70:71], %[aA] offset:2304\ns_waitcnt lgkmcnt(15)\nv_mfma_f32_16x16x32_f16 %[c3], v[4:7], v[52:55], %[c3]\nds_read_b128 v[112:115], %[aB] offset:2112\n" \
  "s_waitcnt lgkmcnt(15)\nv_mfma_f32_16x16x32_f16 %[c2], v[4:7], v[48:51], %[c2]\nds_read_b128 v[96:99], %[aB] offset:64\n" \
  "v_mfma_f32_16x16x32_f16 %[c1], v[4:7], v[36:39], %[c1]\nds_read_b64 v[72:73], %[aA] offset:4384\ns_waitcnt lgkmcnt(15)\n" \
  "v_mfma_f32_16x16x32_f16 %[c0], v[4:7], v[32:35], %[c0]\nds_read_b64 v[74:75], %[aA] offset:4352\ns_waitcnt lgkmcnt(15)\n" \
  "v_mfma_f32_16x16x32_f16 %[c3], v[8:11], v[48:51], %[c3]\nds_read_b128 v[108:111], %[aB] offset:1600\ns_waitcnt lgkmcnt(15)\n" \
  "v_mfma_f32_16x16x32_f16 %[c2], v[8:11], v[44:47], %[c2]\nds_read_b64 v[76:77], %[aA] offset:6432\nv_mfma_f32_16x16x32_f16 %[c1], v[8:11], v[32:35], %[c1]\n" \
  "ds_read_b64 v[78:79], %[aA] offset:6400\ns_waitcnt lgkmcnt(15)\nv_mfma_f32_16x16x32_f16 %[c3], v[12:15], v[44:47], %[c3]\nds_read_b64 v[80:81], %[aA] offset:8480\n" \
  "v_mfma_f32_16x16x32_f16 %[c2], v[12:15], v[40:43], %[c2]\nds_read_b64 v[82:83], %[aA] offset:8448\ns_waitcnt lgkmcnt(15)\n" \
  "v_mfma_f32_16x16x32_f16 %[c3], v[16:19], v[40:43], %[c3]\nds_read_b64 v[84:85], %[aA] offset:10528\nv_mfma_f32_16x16x32_f16 %[c2], v[16:19], v[36:39], %[c2]\n" \
  "ds_read_b64 v[86:87], %[aA] offset:10496\ns_waitcnt lgkmcnt(15)\nv_mfma_f32_16x16x32_f16 %[c3], v[20:23], v[36:39], %[c3]\nds_read_b64 v[88:89], %[aA] offset:12576\n" \
  "v_mfma_f32_16x16x32_f16 %[c2], v[20:23], v[32:35], %[c2]\nds_read_b64 v[90:91], %[aA] offset:12544\ns_waitcnt lgkmcnt(15)\n" \
  "v_mfma_f32_16x16x32_f16 %[c3], v[24:27], v[32:35], %[c3]\ns_sub_u32 %[n], %[n], 1\ns_cmp_eq_u32 %[n], 0\ns_cbranch_scc1 2f\ns_waitcnt lgkmcnt(15)\n" \
  "v_mfma_f32_16x16x32_f16 %[c3], v[64:67], v[120:123], %[c3]\nds_read_b64 v[0:1], %[aA] offset:544\nds_read_b64 v[2:3], %[aA] offset:512\ns_waitcnt lgkmcnt(15)\n" \
  "v_mfma_f32_16x16x32_f16 %[c2], v[64:67], v[116:119], %[c2]\nds_read_b128 v[56:59], %[aB] offset:3072\nds_read_b128 v[52:55], %[aB] offset:2560\ns_waitcnt lgkmcnt(15)\n" \
  "v_mfma_f32_16x16x32_f16 %[c1], v[64:67], v[104:107], %[c1]\nds_read_b128 v[40:43], %[aB] offset:1024\nds_read_b128 v[36:39], %[aB] offset:512\ns_waitcnt lgkmcnt(15)\n" \
  "v_mfma_f32_16x16x32_f16 %[c0], v[64:67], v[100:103], %[c0]\nds_read_b64 v[4:5], %[aA] offset:2592\nds_read_b64 v[6:7], %[aA] offset:2560\ns_waitcnt lgkmcnt(15)\n" \
  "v_mfma_f32_16x16x32_f16 %[c3], v[68:71], v[116:119], %[c3]\nds_read_b128 v[48:51], %[aB] offset:2048\ns_waitcnt lgkmcnt(15)\n" \
  "v_mfma_f32_16x16x32_f16 %[c2], v[68:71], v[112:115], %[c2]\nds_read_b128 v[32:35], %[aB] offset:0\nv_mfma_f32_16x16x32_f16 %[c1], v[68:71], v[100:103], %[c1]\n" \
  "ds_read_b64 v[8:9], %[aA] offset:4640\ns_waitcnt lgkmcnt(15)\nv_mfma_f32_16x16x32_f16 %[c0], v[68:71], v[96:99], %[c0]\nds_read_b64 v[10:11], %[aA] offset:4608\n" \
  "s_waitcnt lgkmcnt(15)\nv_mfma_f32_16x16x32_f16 %[c3], v[72:75], v[112:115], %[c3]\nds_read_b128 v[44:47], %[aB] offset:1536\ns_waitcnt lgkmcnt(15)\n" \
  "v_mfma_f32_16x16x32_f16 %[c2], v[72:75], v[108:111], %[c2]\nds_read_b64 v[12:13], %[aA] offset:6688\nv_mfma_f32_16x16x32_f16 %[c1], v[72:75], v[96:99], %[c1]\n" \
  "ds_read_b64 v[14:15], %[aA] offset:6656\ns_waitcnt lgkmcnt(15)\nv_mfma_f32_16x16x32_f16 %[c3], v[76:79], v[108:111], %[c3]\nds_read_b64 v[16:17], %[aA] offset:8736\n" \
  "v_mfma_f32_16x16x32_f16 %[c2], v[76:79], v[104:107], %[c2]\nds_read_b64 v[18:19], %[aA] offset:8704\ns_waitcnt lgkmcnt(15)\n" \
  "v_mfma_f32_16x16x32_f16 %[c3], v[80:83], v[104:107], %[c3]\nds_read_b64 v[20:21], %[aA] offset:10784\nv_mfma_f32_16x16x32_f16 %[c2], v[80:83], v[100:103], %[c2]\n" \
  "ds_read_b64 v[22:23], %[aA] offset:10752\ns_waitcnt lgkmcnt(15)\nv_mfma_f32_16x16x32_f16 %[c3], v[84:87], v[100:103], %[c3]\nds_read_b64 v[24:25], %[aA] offset:12832\n" \
  "v_mfma_f32_16x16x32_f16 %[c2], v[84:87], v[96:99], %[c2]\nds_read_b64 v[26:27], %[aA] offset:12800\ns_waitcnt lgkmcnt(15)\n" \
  "v_mfma_f32_16x16x32_f16 %[c3], v[88:91], v[96:99], %[c3]\nv_add_u32 %[aA], 0x200, %[aA]\nv_subrev_u32 %[aB], 0x80, %[aB]\ns_branch 1b\n2:\n" \
  "buffer_load_dwordx4 %[pf], %[pa], %[ws], 0 offen sc1\ns_waitcnt lgkmcnt(15)\nv_mfma_f32_16x16x32_f16 %[c3], v[64:67], v[120:123], %[c3]\ns_waitcnt lgkmcnt(15)\n" \
  "v_mfma_f32_16x16x32_f16 %[c2], v[64:67], v[116:119], %[c2]\ns_waitcnt lgkmcnt(15)\nv_mfma_f32_16x16x32_f16 %[c1], v[64:67], v[104:107], %[c1]\ns_waitcnt lgkmcnt(15)\n" \
  "v_mfma_f32_16x16x32_f16 %[c0], v[64:67], v[100:103], %[c0]\ns_waitcnt lgkmcnt(13)\nv_mfma_f32_16x16x32_f16 %[c3], v[68:71], v[116:119], %[c3]\ns_waitcnt lgkmcnt(12)\n" \
  "v_mfma_f32_16x16x32_f16 %[c2], v[68:71], v[112:115], %[c2]\nv_mfma_f32_16x16x32_f16 %[c1], v[68:71], v[100:103], %[c1]\ns_waitcnt lgkmcnt(11)\n" \
  "v_mfma_f32_16x16x32_f16 %[c0], v[68:71], v[96:99], %[c0]\ns_waitcnt lgkmcnt(9)\nv_mfma_f32_16x16x32_f16 %[c3], v[72:75], v[112:115], %[c3]\ns_waitcnt lgkmcnt(8)\n" \
  "v_mfma_f32_16x16x32_f16 %[c2], v[72:75], v[108:111], %[c2]\nv_mfma_f32_16x16x32_f16 %[c1], v[72:75], v[96:99], %[c1]\ns_waitcnt lgkmcnt(6)\n" \
  "v_mfma_f32_16x16x32_f16 %[c3], v[76:79], v[108:111], %[c3]\nv_mfma_f32_16x16x32_f16 %[c2], v[76:79], v[104:107], %[c2]\ns_waitcnt lgkmcnt(4)\n" \
  "v_mfma_f32_16x16x32_f16 %[c3], v[80:83], v[104:107], %[c3]\nv_mfma_f32_16x16x32_f16 %[c2], v[80:83], v[100:103], %[c2]\ns_waitcnt lgkmcnt(2)\n" \
  "v_mfma_f32_16x16x32_f16 %[c3], v[84:87], v[100:103], %[c3]\nv_mfma_f32_16x16x32_f16 %[c2], v[84:87], v[96:99], %[c2]\ns_waitcnt lgkmcnt(0)\n" \
  "v_mfma_f32_16x16x32_f16 %[c3], v[88:91], v[96:99], %[c3]\ns_waitcnt vmcnt(0)\ns_nop 7\n"
#define CONV_CLOB "v0", "v1", "v2", "v3", "v4", "v5", "v6", "v7", "v8", "v9", "v10", "v11", "v12", "v13", "v14", "v15", "v16", "v17", "v18", "v19", "v20", "v21", "v22", "v23", "v24", "v25", "v26", "v27", "v28", "v29", "v30", "v31", "v32", "v33", "v34", "v35", "v36", "v37", "v38", "v39", "v40", "v41", "v42", "v43", "v44", "v45", "v46", "v47", "v48", "v49", "v50", "v51", "v52", "v53", "v54", "v55", "v56", "v57", "v58", "v59", "v60", "v61", "v62", "v63", "v64", "v65", "v66", "v67", "v68", "v69", "v70", "v71", "v72", "v73", "v74", "v75", "v76", "v77", "v78", "v79", "v80", "v81", "v82", "v83", "v84", "v85", "v86", "v87", "v88", "v89", "v90", "v91", "v92", "v93", "v94", "v95", "v96", "v97", "v98", "v99", "v100", "v101", "v102", "v103", "v104", "v105", "v106", "v107", "v108", "v109", "v110", "v111", "v112", "v113", "v114", "v115", "v116", "v117", "v118", "v119", "v120", "v121", "v122", "v123", "v124", "v125", "v126", "v127"
DI u32x4 conv_mma(rsrc_t ws, const Pre& pre, int c, int ts, int r0, int n, f32x4 (&acc)[4]) {
#pragma unroll
  for (int t = 0; t < 4; t++) acc[t] = f32x4{0.f, 0.f, 0.f, 0.f};
  unsigned aa = pre.ca + (c * W4_BYTES + 256 * r0), ba = pre.cb + (c * U_BYTES - 64 * r0);
  u32x4 pf;
#define CONV_OPS : [c0] "+v"(acc[0]), [c1] "+v"(acc[1]), [c2] "+v"(acc[2]), [c3] "+v"(acc[3]), [aA] "+v"(aa), [aB] "+v"(ba), [n] "+s"(n), [pf] "=&v"(pf) \
                 : [pa] "v"(pre.fq), [ws] "s"(ws) : "memory", "scc", CONV_CLOB
  if (ts) asm volatile(CONV_ASM1 CONV_OPS); else asm volatile(CONV_ASM0 CONV_OPS);
  return pf;
}

#define ATTN_ASM \
  "s_mov_b64 s[40:41], %[src]\ns_mov_b32 m0, %[dst]\ns_mov_b32 s48, 0\ns_nop 8\n1:\ns_setprio 1\nv_exp_f32 v0, v0\nv_exp_f32 v1, v1\n" \
  "v_smfmac_f32_32x32x32_bf16 %[OB], v[68:71], v[56:63], %[idx]\nds_read_b64_tr_b16 v[68:69], %[aV] offset:3328\nds_read_b64_tr_b16 v[70:71], %[aV2] offset:3328\n" \
  "ds_read_b64 v[78:79], %[aR] offset:11264\nv_exp_f32 v2, v2\nv_exp_f32 v3, v3\nv_exp_f32 v4, v4\nv_exp_f32 v5, v5\ns_waitcnt lgkmcnt(3)\n" \
  "v_mfma_f32_32x32x16_bf16 v[32:47], v[72:75], %[QmA], %[NA]\nv_exp_f32 v6, v6\nv_exp_f32 v7, v7\nv_exp_f32 v8, v8\nv_exp_f32 v9, v9\n" \
  "v_smfmac_f32_32x32x32_bf16 %[OA], v[64:67], v[0:7], %[idx]\nglobal_load_lds_dwordx4 v80, s[40:41] offset:-3072 sc1\nv_exp_f32 v10, v10\nv_exp_f32 v11, v11\n" \
  "v_exp_f32 v12, v12\nv_exp_f32 v13, v13\nv_mfma_f32_32x32x8_bf16 v[32:47], v[76:77], %[QA1], v[32:47]\nv_exp_f32 v14, v14\nv_exp_f32 v15, v15\nv_exp_f32 v16, v16\n" \
  "v_exp_f32 v17, v17\ns_waitcnt lgkmcnt(1)\nv_smfmac_f32_32x32x32_bf16 %[OA], v[68:71], v[8:15], %[idx]\nv_exp_f32 v18, v18\nv_exp_f32 v19, v19\nv_exp_f32 v20, v20\n" \
  "v_exp_f32 v21, v21\nv_mfma_f32_32x32x16_bf16 v[48:63], v[72:75], %[QmB], %[NB]\nds_read_b128 v[72:75], %[aK] offset:11264\nv_exp_f32 v22, v22\nv_exp_f32 v23, v23\n" \
  "v_exp_f32 v24, v24\nv_exp_f32 v25, v25\nv_smfmac_f32_32x32x32_bf16 %[OB], v[64:67], v[16:23], %[idx]\nds_read_b64_tr_b16 v[64:65], %[aV] offset:4096\n" \
  "ds_read_b64_tr_b16 v[66:67], %[aV2] offset:4096\nv_exp_f32 v26, v26\nv_exp_f32 v27, v27\nv_exp_f32 v28, v28\nv_exp_f32 v29, v29\n" \
  "v_mfma_f32_32x32x8_bf16 v[48:63], v[76:77], %[QB1], v[48:63]\nv_exp_f32 v30, v30\nv_exp_f32 v31, v31\nv_exp_f32 v32, v32\nv_exp_f32 v33, v33\n" \
  "v_smfmac_f32_32x32x32_bf16 %[OB], v[68:71], v[24:31], %[idx]\nds_read_b64_tr_b16 v[68:69], %[aV] offset:4864\nds_read_b64_tr_b16 v[70:71], %[aV2] offset:4864\n" \
  "v_exp_f32 v34, v34\nv_exp_f32 v35, v35\nv_exp_f32 v36, v36\nv_exp_f32 v37, v37\ns_waitcnt lgkmcnt(2)\nv_mfma_f32_32x32x16_bf16 v[0:15], v[72:75], %[QmA], %[NA]\n" \
  "v_exp_f32 v38, v38\nv_exp_f32 v39, v39\nv_exp_f32 v40, v40\nv_exp_f32 v41, v41\nv_smfmac_f32_32x32x32_bf16 %[OA], v[64:67], v[32:39], %[idx]\n" \
  "global_load_lds_dwordx4 v80, s[40:41] offset:-2048 sc1\nv_exp_f32 v42, v42\nv_exp_f32 v43, v43\nv_exp_f32 v44, v44\nv_exp_f32 v45, v45\n" \
  "v_mfma_f32_32x32x8_bf16 v[0:15], v[78:79], %[QA0], v[0:15]\nv_exp_f32 v46, v46\nv_exp_f32 v47, v47\nv_exp_f32 v48, v48\nv_exp_f32 v49, v49\ns_waitcnt lgkmcnt(0)\n" \
  "v_smfmac_f32_32x32x32_bf16 %[OA], v[68:71], v[40:47], %[idx]\nv_exp_f32 v50, v50\nv_exp_f32 v51, v51\nv_exp_f32 v52, v52\nv_exp_f32 v53, v53\n" \
  "v_mfma_f32_32x32x16_bf16 v[16:31], v[72:75], %[QmB], %[NB]\nds_read_b128 v[72:75], %[aK] offset:12288\nv_exp_f32 v54, v54\nv_exp_f32 v55, v55\nv_exp_f32 v56, v56\n" \
  "v_exp_f32 v57, v57\nv_smfmac_f32_32x32x32_bf16 %[OB], v[64:67], v[48:55], %[idx]\nds_read_b64_tr_b16 v[64:65], %[aV] offset:13824\n" \
  "ds_read_b64_tr_b16 v[66:67], %[aV2] offset:13824\nv_exp_f32 v58, v58\nv_exp_f32 v59, v59\nv_exp_f32 v60, v60\nv_exp_f32 v61, v61\n" \
  "v_mfma_f32_32x32x8_bf16 v[16:31], v[78:79], %[QB0], v[16:31]\nv_exp_f32 v62, v62\nv_exp_f32 v63, v63\nv_exp_f32 v0, v0\nv_exp_f32 v1, v1\n" \
  "v_smfmac_f32_32x32x32_bf16 %[OB], v[68:71], v[56:63], %[idx]\nds_read_b64_tr_b16 v[68:69], %[aV] offset:14592\nds_read_b64_tr_b16 v[70:71], %[aV2] offset:14592\n" \
  "ds_read_b64 v[76:77], %[aR] offset:22528\nv_exp_f32 v2, v2\nv_exp_f32 v3, v3\nv_exp_f32 v4, v4\nv_exp_f32 v5, v5\ns_waitcnt lgkmcnt(3)\n" \
  "v_mfma_f32_32x32x16_bf16 v[32:47], v[72:75], %[QmA], %[NA]\nv_exp_f32 v6, v6\nv_exp_f32 v7, v7\nv_exp_f32 v8, v8\nv_exp_f32 v9, v9\n" \
  "v_smfmac_f32_32x32x32_bf16 %[OA], v[64:67], v[0:7], %[idx]\nglobal_load_lds_dwordx4 v80, s[40:41] offset:-1024 sc1\nv_exp_f32 v10, v10\nv_exp_f32 v11, v11\n" \
  "v_exp_f32 v12, v12\nv_exp_f32 v13, v13\nv_mfma_f32_32x32x8_bf16 v[32:47], v[78:79], %[QA1], v[32:47]\nv_exp_f32 v14, v14\nv_exp_f32 v15, v15\nv_exp_f32 v16, v16\n" \
  "v_exp_f32 v17, v17\ns_waitcnt lgkmcnt(1)\nv_smfmac_f32_32x32x32_bf16 %[OA], v[68:71], v[8:15], %[idx]\nv_exp_f32 v18, v18\nv_exp_f32 v19, v19\nv_exp_f32 v20, v20\n" \
  "v_exp_f32 v21, v21\nv_mfma_f32_32x32x16_bf16 v[48:63], v[72:75], %[QmB], %[NB]\nds_read_b128 v[72:75], %[aK] offset:22528\nv_exp_f32 v22, v22\nv_exp_f32 v23, v23\n" \
  "v_exp_f32 v24, v24\nv_exp_f32 v25, v25\nv_smfmac_f32_32x32x32_bf16 %[OB], v[64:67], v[16:23], %[idx]\nds_read_b64_tr_b16 v[64:65], %[aV] offset:15360\n" \
  "ds_read_b64_tr_b16 v[66:67], %[aV2] offset:15360\nv_exp_f32 v26, v26\nv_exp_f32 v27, v27\nv_exp_f32 v28, v28\nv_exp_f32 v29, v29\n" \
  "v_mfma_f32_32x32x8_bf16 v[48:63], v[78:79], %[QB1], v[48:63]\nv_exp_f32 v30, v30\nv_exp_f32 v31, v31\ns_setprio 0\nv_exp_f32 v32, v32\nv_exp_f32 v33, v33\n" \
  "v_smfmac_f32_32x32x32_bf16 %[OB], v[68:71], v[24:31], %[idx]\nds_read_b64_tr_b16 v[68:69], %[aV] offset:16128\nds_read_b64_tr_b16 v[70:71], %[aV2] offset:16128\n" \
  "v_exp_f32 v34, v34\nv_exp_f32 v35, v35\nv_exp_f32 v36, v36\nv_exp_f32 v37, v37\ns_waitcnt lgkmcnt(2)\nv_mfma_f32_32x32x16_bf16 v[0:15], v[72:75], %[QmA], %[NA]\n" \
  "v_exp_f32 v38, v38\nv_exp_f32 v39, v39\nv_exp_f32 v40, v40\nv_exp_f32 v41, v41\nv_smfmac_f32_32x32x32_bf16 %[OA], v[64:67], v[32:39], %[idx]\n" \
  "global_load_lds_dwordx4 v80, s[40:41] offset:0 sc1\nv_exp_f32 v42, v42\nv_exp_f32 v43, v43\nv_exp_f32 v44, v44\nv_exp_f32 v45, v45\n" \
  "v_mfma_f32_32x32x8_bf16 v[0:15], v[76:77], %[QA0], v[0:15]\nv_exp_f32 v46, v46\nv_exp_f32 v47, v47\nv_exp_f32 v48, v48\nv_exp_f32 v49, v49\ns_waitcnt lgkmcnt(0)\n" \
  "v_smfmac_f32_32x32x32_bf16 %[OA], v[68:71], v[40:47], %[idx]\nv_exp_f32 v50, v50\nv_exp_f32 v51, v51\nv_exp_f32 v52, v52\nv_exp_f32 v53, v53\n" \
  "v_mfma_f32_32x32x16_bf16 v[16:31], v[72:75], %[QmB], %[NB]\nds_read_b128 v[72:75], %[aK] offset:23552\nv_exp_f32 v54, v54\nv_exp_f32 v55, v55\nv_exp_f32 v56, v56\n" \
  "v_exp_f32 v57, v57\nv_smfmac_f32_32x32x32_bf16 %[OB], v[64:67], v[48:55], %[idx]\nds_read_b64_tr_b16 v[64:65], %[aV] offset:25088\n" \
  "ds_read_b64_tr_b16 v[66:67], %[aV2] offset:25088\nv_exp_f32 v58, v58\nv_exp_f32 v59, v59\nv_exp_f32 v60, v60\nv_exp_f32 v61, v61\n" \
  "v_mfma_f32_32x32x8_bf16 v[16:31], v[76:77], %[QB0], v[16:31]\nv_exp_f32 v62, v62\nv_exp_f32 v63, v63\ns_setprio 1\nv_exp_f32 v0, v0\nv_exp_f32 v1, v1\n" \
  "v_smfmac_f32_32x32x32_bf16 %[OB], v[68:71], v[56:63], %[idx]\nds_read_b64_tr_b16 v[68:69], %[aV] offset:25856\nds_read_b64_tr_b16 v[70:71], %[aV2] offset:25856\n" \
  "ds_read_b64 v[78:79], %[aR] offset:33792\nv_exp_f32 v2, v2\nv_exp_f32 v3, v3\nv_exp_f32 v4, v4\nv_exp_f32 v5, v5\ns_waitcnt lgkmcnt(3)\n" \
  "v_mfma_f32_32x32x16_bf16 v[32:47], v[72:75], %[QmA], %[NA]\nv_exp_f32 v6, v6\nv_exp_f32 v7, v7\nv_exp_f32 v8, v8\nv_exp_f32 v9, v9\n" \
  "v_smfmac_f32_32x32x32_bf16 %[OA], v[64:67], v[0:7], %[idx]\nglobal_load_lds_dwordx4 v80, s[40:41] offset:1024 sc1\nv_exp_f32 v10, v10\nv_exp_f32 v11, v11\n" \
  "v_exp_f32 v12, v12\nv_exp_f32 v13, v13\nv_mfma_f32_32x32x8_bf16 v[32:47], v[76:77], %[QA1], v[32:47]\nv_exp_f32 v14, v14\nv_exp_f32 v15, v15\nv_exp_f32 v16, v16\n" \
  "v_exp_f32 v17, v17\ns_waitcnt lgkmcnt(1)\nv_smfmac_f32_32x32x32_bf16 %[OA], v[68:71], v[8:15], %[idx]\nv_exp_f32 v18, v18\nv_exp_f32 v19, v19\nv_exp_f32 v20, v20\n" \
  "v_exp_f32 v21, v21\nv_mfma_f32_32x32x16_bf16 v[48:63], v[72:75], %[QmB], %[NB]\nds_read_b128 v[72:75], %[aK] offset:33792\nv_exp_f32 v22, v22\nv_exp_f32 v23, v23\n" \
  "v_exp_f32 v24, v24\nv_exp_f32 v25, v25\nv_smfmac_f32_32x32x32_bf16 %[OB], v[64:67], v[16:23], %[idx]\nds_read_b64_tr_b16 v[64:65], %[aV] offset:26624\n" \
  "ds_read_b64_tr_b16 v[66:67], %[aV2] offset:26624\nv_exp_f32 v26, v26\nv_exp_f32 v27, v27\nv_exp_f32 v28, v28\nv_exp_f32 v29, v29\n" \
  "v_mfma_f32_32x32x8_bf16 v[48:63], v[76:77], %[QB1], v[48:63]\nv_exp_f32 v30, v30\nv_exp_f32 v31, v31\nv_exp_f32 v32, v32\nv_exp_f32 v33, v33\n" \
  "v_smfmac_f32_32x32x32_bf16 %[OB], v[68:71], v[24:31], %[idx]\nds_read_b64_tr_b16 v[68:69], %[aV] offset:27392\nds_read_b64_tr_b16 v[70:71], %[aV2] offset:27392\n" \
  "v_exp_f32 v34, v34\nv_exp_f32 v35, v35\nv_exp_f32 v36, v36\nv_exp_f32 v37, v37\ns_waitcnt lgkmcnt(2)\nv_mfma_f32_32x32x16_bf16 v[0:15], v[72:75], %[QmA], %[NA]\n" \
  "v_exp_f32 v38, v38\nv_exp_f32 v39, v39\nv_exp_f32 v40, v40\nv_exp_f32 v41, v41\nv_smfmac_f32_32x32x32_bf16 %[OA], v[64:67], v[32:39], %[idx]\n" \
  "global_load_lds_dwordx4 v80, s[40:41] offset:2048 sc1\nv_exp_f32 v42, v42\nv_exp_f32 v43, v43\nv_exp_f32 v44, v44\nv_exp_f32 v45, v45\n" \
  "v_mfma_f32_32x32x8_bf16 v[0:15], v[78:79], %[QA0], v[0:15]\nv_exp_f32 v46, v46\nv_exp_f32 v47, v47\nv_exp_f32 v48, v48\nv_exp_f32 v49, v49\ns_waitcnt lgkmcnt(0)\n" \
  "v_smfmac_f32_32x32x32_bf16 %[OA], v[68:71], v[40:47], %[idx]\nv_exp_f32 v50, v50\nv_exp_f32 v51, v51\nv_exp_f32 v52, v52\nv_exp_f32 v53, v53\n" \
  "v_mfma_f32_32x32x16_bf16 v[16:31], v[72:75], %[QmB], %[NB]\nds_read_b128 v[72:75], %[aK] offset:34816\nv_exp_f32 v54, v54\nv_exp_f32 v55, v55\nv_exp_f32 v56, v56\n" \
  "v_exp_f32 v57, v57\nv_smfmac_f32_32x32x32_bf16 %[OB], v[64:67], v[48:55], %[idx]\nds_read_b64_tr_b16 v[64:65], %[aV] offset:36352\n" \
  "ds_read_b64_tr_b16 v[66:67], %[aV2] offset:36352\nv_exp_f32 v58, v58\nv_exp_f32 v59, v59\nv_exp_f32 v60, v60\nv_exp_f32 v61, v61\n" \
  "v_mfma_f32_32x32x8_bf16 v[16:31], v[78:79], %[QB0], v[16:31]\nv_exp_f32 v62, v62\nv_exp_f32 v63, v63\nv_exp_f32 v0, v0\nv_exp_f32 v1, v1\n" \
  "v_smfmac_f32_32x32x32_bf16 %[OB], v[68:71], v[56:63], %[idx]\nds_read_b64_tr_b16 v[68:69], %[aV] offset:37120\nds_read_b64_tr_b16 v[70:71], %[aV2] offset:37120\n" \
  "ds_read_b64 v[76:77], %[aR] offset:45056\nv_exp_f32 v2, v2\nv_exp_f32 v3, v3\nv_exp_f32 v4, v4\nv_exp_f32 v5, v5\ns_waitcnt lgkmcnt(3)\n" \
  "v_mfma_f32_32x32x16_bf16 v[32:47], v[72:75], %[QmA], %[NA]\nv_exp_f32 v6, v6\nv_exp_f32 v7, v7\nv_exp_f32 v8, v8\nv_exp_f32 v9, v9\n" \
  "v_smfmac_f32_32x32x32_bf16 %[OA], v[64:67], v[0:7], %[idx]\nv_exp_f32 v10, v10\nv_exp_f32 v11, v11\nv_exp_f32 v12, v12\nv_exp_f32 v13, v13\n" \
  "v_mfma_f32_32x32x8_bf16 v[32:47], v[78:79], %[QA1], v[32:47]\nv_exp_f32 v14, v14\nv_exp_f32 v15, v15\nv_exp_f32 v16, v16\nv_exp_f32 v17, v17\ns_waitcnt lgkmcnt(1)\n" \
  "v_smfmac_f32_32x32x32_bf16 %[OA], v[68:71], v[8:15], %[idx]\nv_exp_f32 v18, v18\nv_exp_f32 v19, v19\nv_exp_f32 v20, v20\nv_exp_f32 v21, v21\n" \
  "v_mfma_f32_32x32x16_bf16 v[48:63], v[72:75], %[QmB], %[NB]\nds_read_b128 v[72:75], %[aK] offset:45056\nv_exp_f32 v22, v22\nv_exp_f32 v23, v23\nv_exp_f32 v24, v24\n" \
  "v_exp_f32 v25, v25\nv_smfmac_f32_32x32x32_bf16 %[OB], v[64:67], v[16:23], %[idx]\nds_read_b64_tr_b16 v[64:65], %[aV] offset:37888\n" \
  "ds_read_b64_tr_b16 v[66:67], %[aV2] offset:37888\nv_exp_f32 v26, v26\nv_exp_f32 v27, v27\nv_exp_f32 v28, v28\nv_exp_f32 v29, v29\n" \
  "v_mfma_f32_32x32x8_bf16 v[48:63], v[78:79], %[QB1], v[48:63]\nv_exp_f32 v30, v30\nv_exp_f32 v31, v31\ns_setprio 0\nv_exp_f32 v32, v32\nv_exp_f32 v33, v33\n" \
  "v_smfmac_f32_32x32x32_bf16 %[OB], v[68:71], v[24:31], %[idx]\nds_read_b64_tr_b16 v[68:69], %[aV] offset:38656\nds_read_b64_tr_b16 v[70:71], %[aV2] offset:38656\n" \
  "v_exp_f32 v34, v34\nv_exp_f32 v35, v35\nv_exp_f32 v36, v36\nv_exp_f32 v37, v37\ns_waitcnt lgkmcnt(2)\nv_mfma_f32_32x32x16_bf16 v[0:15], v[72:75], %[QmA], %[NA]\n" \
  "v_exp_f32 v38, v38\nv_exp_f32 v39, v39\nv_exp_f32 v40, v40\nv_exp_f32 v41, v41\nv_smfmac_f32_32x32x32_bf16 %[OA], v[64:67], v[32:39], %[idx]\nv_exp_f32 v42, v42\n" \
  "v_exp_f32 v43, v43\nv_exp_f32 v44, v44\nv_exp_f32 v45, v45\nv_mfma_f32_32x32x8_bf16 v[0:15], v[76:77], %[QA0], v[0:15]\nv_exp_f32 v46, v46\nv_exp_f32 v47, v47\n" \
  "v_exp_f32 v48, v48\nv_exp_f32 v49, v49\ns_waitcnt lgkmcnt(0)\nv_smfmac_f32_32x32x32_bf16 %[OA], v[68:71], v[40:47], %[idx]\nv_exp_f32 v50, v50\nv_exp_f32 v51, v51\n" \
  "v_exp_f32 v52, v52\nv_exp_f32 v53, v53\nv_mfma_f32_32x32x16_bf16 v[16:31], v[72:75], %[QmB], %[NB]\nds_read_b128 v[72:75], %[aK] offset:46080\nv_exp_f32 v54, v54\n" \
  "v_exp_f32 v55, v55\nv_exp_f32 v56, v56\nv_exp_f32 v57, v57\nv_smfmac_f32_32x32x32_bf16 %[OB], v[64:67], v[48:55], %[idx]\n" \
  "ds_read_b64_tr_b16 v[64:65], %[aV] offset:47616\nds_read_b64_tr_b16 v[66:67], %[aV2] offset:47616\nv_exp_f32 v58, v58\nv_exp_f32 v59, v59\nv_exp_f32 v60, v60\n" \
  "v_exp_f32 v61, v61\nv_mfma_f32_32x32x8_bf16 v[16:31], v[76:77], %[QB0], v[16:31]\nv_exp_f32 v62, v62\nv_exp_f32 v63, v63\ns_add_u32 s48, s48, 1\ns_cmp_eq_u32 s48, 3\n" \
  "s_waitcnt vmcnt(0) lgkmcnt(0)\ns_barrier\ns_cbranch_scc1 3f\nv_add_u32 %[aK], 45056, %[aK]\nv_add_u32 %[aR], 45056, %[aR]\nv_add_u32 %[aV], 45056, %[aV]\n" \
  "v_add_u32 %[aV2], 45056, %[aV2]\ns_cmp_eq_u32 s48, 1\ns_cbranch_scc0 2f\ns_add_u32 s40, s40, 45056\ns_addc_u32 s41, s41, 0\ns_add_u32 m0, m0, 45056\n" \
  "s_cmp_lg_u32 %[wv], 7\ns_cbranch_scc1 1b\ns_mov_b64 exec, 1\nbuffer_store_dwordx2 %[tag], off, %[ws], %[fym] sc1\ns_mov_b64 exec, -1\ns_branch 1b\n2:\n" \
  "buffer_load_dwordx2 v[96:97], %[fyo], %[ws], 0 offen sc1\ns_add_u32 s40, s40, 33792\ns_addc_u32 s41, s41, 0\ns_sub_u32 m0, m0, 101376\ns_branch 1b\n3:\n" \
  "s_mov_b32 s48, 0x10000\n4:\nv_cmp_ne_u64 vcc, v[96:97], %[tag]\ns_sub_u32 s48, s48, 1\ns_nop 3\ns_cbranch_vccz 5f\ns_cbranch_scc1 5f\ns_sleep 1\n" \
  "buffer_load_dwordx2 v[96:97], %[fyo], %[ws], 0 offen sc1\ns_waitcnt vmcnt(0)\ns_branch 4b\n5:\ns_mov_b32 m0, %[yd0]\nv_add_u32 %[aK], 0xfffea000, %[aK]\n" \
  "v_add_u32 %[aR], 0xfffea000, %[aR]\nv_add_u32 %[aV], 0xfffea000, %[aV]\nv_add_u32 %[aV2], 0xfffea000, %[aV2]\nglobal_load_lds_dwordx4 v93, %[ys0] sc1\n" \
  "s_mov_b32 m0, %[yd1]\ns_nop 0\nglobal_load_lds_dwordx4 v93, %[ys1] sc1\ns_mov_b64 s[40:41], %[wsrc]\ns_mov_b32 m0, %[wdst]\ns_cmp_lt_u32 %[wv], 4\n" \
  "s_cselect_b64 vcc, 1, 0\ns_setprio 1\nv_exp_f32 v0, v0\nv_exp_f32 v1, v1\nv_smfmac_f32_32x32x32_bf16 %[OB], v[68:71], v[56:63], %[idx]\n" \
  "ds_read_b64_tr_b16 v[68:69], %[aV] offset:3328\nds_read_b64_tr_b16 v[70:71], %[aV2] offset:3328\nds_read_b64 v[78:79], %[aR] offset:11264\nv_exp_f32 v2, v2\n" \
  "v_exp_f32 v3, v3\nv_exp_f32 v4, v4\nv_exp_f32 v5, v5\ns_waitcnt lgkmcnt(3)\nv_mfma_f32_32x32x16_bf16 v[32:47], v[72:75], %[QmA], %[NA]\nv_exp_f32 v6, v6\n" \
  "v_exp_f32 v7, v7\nv_exp_f32 v8, v8\nv_exp_f32 v9, v9\nv_smfmac_f32_32x32x32_bf16 %[OA], v[64:67], v[0:7], %[idx]\nglobal_load_lds_dwordx4 v88, s[40:41] offset:-3072 sc1\n" \
  "v_exp_f32 v10, v10\nv_exp_f32 v11, v11\nv_exp_f32 v12, v12\nv_exp_f32 v13, v13\nv_mfma_f32_32x32x8_bf16 v[32:47], v[76:77], %[QA1], v[32:47]\nv_exp_f32 v14, v14\n" \
  "v_exp_f32 v15, v15\nv_exp_f32 v16, v16\nv_exp_f32 v17, v17\ns_waitcnt lgkmcnt(1)\nv_smfmac_f32_32x32x32_bf16 %[OA], v[68:71], v[8:15], %[idx]\n" \
  "global_load_lds_dwordx4 v89, s[40:41] offset:-2048 sc1\nv_exp_f32 v18, v18\nv_exp_f32 v19, v19\nv_exp_f32 v20, v20\nv_exp_f32 v21, v21\n" \
  "v_mfma_f32_32x32x16_bf16 v[48:63], v[72:75], %[QmB], %[NB]\nds_read_b128 v[72:75], %[aK] offset:11264\nv_exp_f32 v22, v22\nv_exp_f32 v23, v23\nv_exp_f32 v24, v24\n" \
  "v_exp_f32 v25, v25\nv_smfmac_f32_32x32x32_bf16 %[OB], v[64:67], v[16:23], %[idx]\nds_read_b64_tr_b16 v[64:65], %[aV] offset:4096\n" \
  "ds_read_b64_tr_b16 v[66:67], %[aV2] offset:4096\nv_exp_f32 v26, v26\nv_exp_f32 v27, v27\nv_exp_f32 v28, v28\nv_exp_f32 v29, v29\n" \
  "v_mfma_f32_32x32x8_bf16 v[48:63], v[76:77], %[QB1], v[48:63]\nv_exp_f32 v30, v30\nv_exp_f32 v31, v31\nv_exp_f32 v32, v32\nv_exp_f32 v33, v33\n" \
  "v_smfmac_f32_32x32x32_bf16 %[OB], v[68:71], v[24:31], %[idx]\nds_read_b64_tr_b16 v[68:69], %[aV] offset:4864\nds_read_b64_tr_b16 v[70:71], %[aV2] offset:4864\n" \
  "v_exp_f32 v34, v34\nv_exp_f32 v35, v35\nv_exp_f32 v36, v36\nv_exp_f32 v37, v37\ns_waitcnt lgkmcnt(2)\nv_mfma_f32_32x32x16_bf16 v[0:15], v[72:75], %[QmA], %[NA]\n" \
  "v_exp_f32 v38, v38\nv_exp_f32 v39, v39\nv_exp_f32 v40, v40\nv_exp_f32 v41, v41\nv_smfmac_f32_32x32x32_bf16 %[OA], v[64:67], v[32:39], %[idx]\n" \
  "global_load_lds_dwordx4 v90, s[40:41] offset:-1024 sc1\nv_exp_f32 v42, v42\nv_exp_f32 v43, v43\nv_exp_f32 v44, v44\nv_exp_f32 v45, v45\n" \
  "v_mfma_f32_32x32x8_bf16 v[0:15], v[78:79], %[QA0], v[0:15]\nv_exp_f32 v46, v46\nv_exp_f32 v47, v47\nv_exp_f32 v48, v48\nv_exp_f32 v49, v49\ns_waitcnt lgkmcnt(0)\n" \
  "v_smfmac_f32_32x32x32_bf16 %[OA], v[68:71], v[40:47], %[idx]\nglobal_load_lds_dwordx4 v91, s[40:41] offset:0 sc1\nv_exp_f32 v50, v50\nv_exp_f32 v51, v51\n" \
  "v_exp_f32 v52, v52\nv_exp_f32 v53, v53\nv_mfma_f32_32x32x16_bf16 v[16:31], v[72:75], %[QmB], %[NB]\nds_read_b128 v[72:75], %[aK] offset:12288\nv_exp_f32 v54, v54\n" \
  "v_exp_f32 v55, v55\nv_exp_f32 v56, v56\nv_exp_f32 v57, v57\nv_smfmac_f32_32x32x32_bf16 %[OB], v[64:67], v[48:55], %[idx]\n" \
  "ds_read_b64_tr_b16 v[64:65], %[aV] offset:13824\nds_read_b64_tr_b16 v[66:67], %[aV2] offset:13824\nv_exp_f32 v58, v58\nv_exp_f32 v59, v59\nv_exp_f32 v60, v60\n" \
  "v_exp_f32 v61, v61\nv_mfma_f32_32x32x8_bf16 v[16:31], v[78:79], %[QB0], v[16:31]\nv_exp_f32 v62, v62\nv_exp_f32 v63, v63\nv_exp_f32 v0, v0\nv_exp_f32 v1, v1\n" \
  "v_smfmac_f32_32x32x32_bf16 %[OB], v[68:71], v[56:63], %[idx]\nds_read_b64_tr_b16 v[68:69], %[aV] offset:14592\nds_read_b64_tr_b16 v[70:71], %[aV2] offset:14592\n" \
  "ds_read_b64 v[76:77], %[aR] offset:22528\nv_exp_f32 v2, v2\nv_exp_f32 v3, v3\nv_exp_f32 v4, v4\nv_exp_f32 v5, v5\ns_waitcnt lgkmcnt(3)\n" \
  "v_mfma_f32_32x32x16_bf16 v[32:47], v[72:75], %[QmA], %[NA]\nv_exp_f32 v6, v6\nv_exp_f32 v7, v7\nv_exp_f32 v8, v8\nv_exp_f32 v9, v9\n" \
  "v_smfmac_f32_32x32x32_bf16 %[OA], v[64:67], v[0:7], %[idx]\nglobal_load_lds_dwordx4 v92, s[40:41] offset:1024 sc1\nv_exp_f32 v10, v10\nv_exp_f32 v11, v11\n" \
  "v_exp_f32 v12, v12\nv_exp_f32 v13, v13\nv_mfma_f32_32x32x8_bf16 v[32:47], v[78:79], %[QA1], v[32:47]\nv_exp_f32 v14, v14\nv_exp_f32 v15, v15\nv_exp_f32 v16, v16\n" \
  "v_exp_f32 v17, v17\ns_waitcnt lgkmcnt(1)\nv_smfmac_f32_32x32x32_bf16 %[OA], v[68:71], v[8:15], %[idx]\ns_add_u32 m0, m0, 40960\ns_add_u32 s40, s40, 327680\n" \
  "s_addc_u32 s41, s41, 0\nglobal_load_lds_dwordx4 v88, s[40:41] offset:-3072 sc1\nv_exp_f32 v18, v18\nv_exp_f32 v19, v19\nv_exp_f32 v20, v20\nv_exp_f32 v21, v21\n" \
  "v_mfma_f32_32x32x16_bf16 v[48:63], v[72:75], %[QmB], %[NB]\nds_read_b128 v[72:75], %[aK] offset:22528\nv_exp_f32 v22, v22\nv_exp_f32 v23, v23\nv_exp_f32 v24, v24\n" \
  "v_exp_f32 v25, v25\nv_smfmac_f32_32x32x32_bf16 %[OB], v[64:67], v[16:23], %[idx]\nds_read_b64_tr_b16 v[64:65], %[aV] offset:15360\n" \
  "ds_read_b64_tr_b16 v[66:67], %[aV2] offset:15360\nv_exp_f32 v26, v26\nv_exp_f32 v27, v27\nv_exp_f32 v28, v28\nv_exp_f32 v29, v29\n" \
  "v_mfma_f32_32x32x8_bf16 v[48:63], v[78:79], %[QB1], v[48:63]\nv_exp_f32 v30, v30\nv_exp_f32 v31, v31\ns_setprio 0\nv_exp_f32 v32, v32\nv_exp_f32 v33, v33\n" \
  "v_smfmac_f32_32x32x32_bf16 %[OB], v[68:71], v[24:31], %[idx]\nds_read_b64_tr_b16 v[68:69], %[aV] offset:16128\nds_read_b64_tr_b16 v[70:71], %[aV2] offset:16128\n" \
  "v_exp_f32 v34, v34\nv_exp_f32 v35, v35\nv_exp_f32 v36, v36\nv_exp_f32 v37, v37\ns_waitcnt lgkmcnt(2)\nv_mfma_f32_32x32x16_bf16 v[0:15], v[72:75], %[QmA], %[NA]\n" \
  "v_exp_f32 v38, v38\nv_exp_f32 v39, v39\nv_exp_f32 v40, v40\nv_exp_f32 v41, v41\nv_smfmac_f32_32x32x32_bf16 %[OA], v[64:67], v[32:39], %[idx]\n" \
  "global_load_lds_dwordx4 v89, s[40:41] offset:-2048 sc1\nv_exp_f32 v42, v42\nv_exp_f32 v43, v43\nv_exp_f32 v44, v44\nv_exp_f32 v45, v45\n" \
  "v_mfma_f32_32x32x8_bf16 v[0:15], v[76:77], %[QA0], v[0:15]\nv_exp_f32 v46, v46\nv_exp_f32 v47, v47\nv_exp_f32 v48, v48\nv_exp_f32 v49, v49\ns_waitcnt lgkmcnt(0)\n" \
  "v_smfmac_f32_32x32x32_bf16 %[OA], v[68:71], v[40:47], %[idx]\nglobal_load_lds_dwordx4 v90, s[40:41] offset:-1024 sc1\nv_exp_f32 v50, v50\nv_exp_f32 v51, v51\n" \
  "v_exp_f32 v52, v52\nv_exp_f32 v53, v53\nv_mfma_f32_32x32x16_bf16 v[16:31], v[72:75], %[QmB], %[NB]\nds_read_b128 v[72:75], %[aK] offset:23552\nv_exp_f32 v54, v54\n" \
  "v_exp_f32 v55, v55\nv_exp_f32 v56, v56\nv_exp_f32 v57, v57\nv_smfmac_f32_32x32x32_bf16 %[OB], v[64:67], v[48:55], %[idx]\n" \
  "ds_read_b64_tr_b16 v[64:65], %[aV] offset:25088\nds_read_b64_tr_b16 v[66:67], %[aV2] offset:25088\nv_exp_f32 v58, v58\nv_exp_f32 v59, v59\nv_exp_f32 v60, v60\n" \
  "v_exp_f32 v61, v61\nv_mfma_f32_32x32x8_bf16 v[16:31], v[76:77], %[QB0], v[16:31]\nv_exp_f32 v62, v62\nv_exp_f32 v63, v63\ns_setprio 1\nv_exp_f32 v0, v0\n" \
  "v_exp_f32 v1, v1\nv_smfmac_f32_32x32x32_bf16 %[OB], v[68:71], v[56:63], %[idx]\nds_read_b64_tr_b16 v[68:69], %[aV] offset:25856\n" \
  "ds_read_b64_tr_b16 v[70:71], %[aV2] offset:25856\nds_read_b64 v[78:79], %[aR] offset:33792\nv_exp_f32 v2, v2\nv_exp_f32 v3, v3\nv_exp_f32 v4, v4\nv_exp_f32 v5, v5\n" \
  "s_waitcnt lgkmcnt(3)\nv_mfma_f32_32x32x16_bf16 v[32:47], v[72:75], %[QmA], %[NA]\nv_exp_f32 v6, v6\nv_exp_f32 v7, v7\nv_exp_f32 v8, v8\nv_exp_f32 v9, v9\n" \
  "v_smfmac_f32_32x32x32_bf16 %[OA], v[64:67], v[0:7], %[idx]\nglobal_load_lds_dwordx4 v91, s[40:41] offset:0 sc1\nv_exp_f32 v10, v10\nv_exp_f32 v11, v11\n" \
  "v_exp_f32 v12, v12\nv_exp_f32 v13, v13\nv_mfma_f32_32x32x8_bf16 v[32:47], v[76:77], %[QA1], v[32:47]\nv_exp_f32 v14, v14\nv_exp_f32 v15, v15\nv_exp_f32 v16, v16\n" \
  "v_exp_f32 v17, v17\ns_waitcnt lgkmcnt(1)\nv_smfmac_f32_32x32x32_bf16 %[OA], v[68:71], v[8:15], %[idx]\nglobal_load_lds_dwordx4 v92, s[40:41] offset:1024 sc1\n" \
  "v_exp_f32 v18, v18\nv_exp_f32 v19, v19\nv_exp_f32 v20, v20\nv_exp_f32 v21, v21\nv_mfma_f32_32x32x16_bf16 v[48:63], v[72:75], %[QmB], %[NB]\n" \
  "ds_read_b128 v[72:75], %[aK] offset:33792\nv_exp_f32 v22, v22\nv_exp_f32 v23, v23\nv_exp_f32 v24, v24\nv_exp_f32 v25, v25\n" \
  "v_smfmac_f32_32x32x32_bf16 %[OB], v[64:67], v[16:23], %[idx]\nds_read_b64_tr_b16 v[64:65], %[aV] offset:26624\nds_read_b64_tr_b16 v[66:67], %[aV2] offset:26624\n" \
  "v_exp_f32 v26, v26\nv_exp_f32 v27, v27\nv_exp_f32 v28, v28\nv_exp_f32 v29, v29\nv_mfma_f32_32x32x8_bf16 v[48:63], v[76:77], %[QB1], v[48:63]\nv_exp_f32 v30, v30\n" \
  "v_exp_f32 v31, v31\nv_exp_f32 v32, v32\nv_exp_f32 v33, v33\nv_smfmac_f32_32x32x32_bf16 %[OB], v[68:71], v[24:31], %[idx]\n" \
  "ds_read_b64_tr_b16 v[68:69], %[aV] offset:27392\nds_read_b64_tr_b16 v[70:71], %[aV2] offset:27392\nv_exp_f32 v34, v34\nv_exp_f32 v35, v35\nv_exp_f32 v36, v36\n" \
  "v_exp_f32 v37, v37\ns_waitcnt lgkmcnt(2)\nv_mfma_f32_32x32x16_bf16 v[0:15], v[72:75], %[QmA], %[NA]\nv_exp_f32 v38, v38\nv_exp_f32 v39, v39\nv_exp_f32 v40, v40\n" \
  "v_exp_f32 v41, v41\nv_smfmac_f32_32x32x32_bf16 %[OA], v[64:67], v[32:39], %[idx]\ns_cbranch_vccz 9f\ns_add_u32 m0, m0, 40960\ns_add_u32 s40, s40, 327680\n" \
  "s_addc_u32 s41, s41, 0\nglobal_load_lds_dwordx4 v88, s[40:41] offset:-3072 sc1\nglobal_load_lds_dwordx4 v89, s[40:41] offset:-2048 sc1\n9:\nv_exp_f32 v42, v42\n" \
  "v_exp_f32 v43, v43\nv_exp_f32 v44, v44\nv_exp_f32 v45, v45\nv_mfma_f32_32x32x8_bf16 v[0:15], v[78:79], %[QA0], v[0:15]\nv_exp_f32 v46, v46\nv_exp_f32 v47, v47\n" \
  "v_exp_f32 v48, v48\nv_exp_f32 v49, v49\ns_waitcnt lgkmcnt(0)\nv_smfmac_f32_32x32x32_bf16 %[OA], v[68:71], v[40:47], %[idx]\nv_exp_f32 v50, v50\nv_exp_f32 v51, v51\n" \
  "v_exp_f32 v52, v52\nv_exp_f32 v53, v53\nv_mfma_f32_32x32x16_bf16 v[16:31], v[72:75], %[QmB], %[NB]\nds_read_b128 v[72:75], %[aK] offset:34816\nv_exp_f32 v54, v54\n" \
  "v_exp_f32 v55, v55\nv_exp_f32 v56, v56\nv_exp_f32 v57, v57\nv_smfmac_f32_32x32x32_bf16 %[OB], v[64:67], v[48:55], %[idx]\n" \
  "ds_read_b64_tr_b16 v[64:65], %[aV] offset:36352\nds_read_b64_tr_b16 v[66:67], %[aV2] offset:36352\nv_exp_f32 v58, v58\nv_exp_f32 v59, v59\nv_exp_f32 v60, v60\n" \
  "v_exp_f32 v61, v61\nv_mfma_f32_32x32x8_bf16 v[16:31], v[78:79], %[QB0], v[16:31]\nv_exp_f32 v62, v62\nv_exp_f32 v63, v63\nv_exp_f32 v0, v0\nv_exp_f32 v1, v1\n" \
  "v_smfmac_f32_32x32x32_bf16 %[OB], v[68:71], v[56:63], %[idx]\nds_read_b64_tr_b16 v[68:69], %[aV] offset:37120\nds_read_b64_tr_b16 v[70:71], %[aV2] offset:37120\n" \
  "v_exp_f32 v2, v2\nv_exp_f32 v3, v3\nv_exp_f32 v4, v4\nv_exp_f32 v5, v5\ns_waitcnt lgkmcnt(2)\nv_mfma_f32_32x32x16_bf16 v[32:47], v[72:75], %[QmA], %[NA]\n" \
  "v_exp_f32 v6, v6\nv_exp_f32 v7, v7\nv_exp_f32 v8, v8\nv_exp_f32 v9, v9\nv_smfmac_f32_32x32x32_bf16 %[OA], v[64:67], v[0:7], %[idx]\ns_cbranch_vccz 9f\n" \
  "global_load_lds_dwordx4 v90, s[40:41] offset:-1024 sc1\nglobal_load_lds_dwordx4 v91, s[40:41] offset:0 sc1\nglobal_load_lds_dwordx4 v92, s[40:41] offset:1024 sc1\n9:\n" \
  "v_exp_f32 v10, v10\nv_exp_f32 v11, v11\nv_exp_f32 v12, v12\nv_exp_f32 v13, v13\nv_mfma_f32_32x32x8_bf16 v[32:47], v[78:79], %[QA1], v[32:47]\nv_exp_f32 v14, v14\n" \
  "v_exp_f32 v15, v15\nv_exp_f32 v16, v16\nv_exp_f32 v17, v17\ns_waitcnt lgkmcnt(0)\nv_smfmac_f32_32x32x32_bf16 %[OA], v[68:71], v[8:15], %[idx]\nv_exp_f32 v18, v18\n" \
  "v_exp_f32 v19, v19\nv_exp_f32 v20, v20\nv_exp_f32 v21, v21\nv_mfma_f32_32x32x16_bf16 v[48:63], v[72:75], %[QmB], %[NB]\nv_exp_f32 v22, v22\nv_exp_f32 v23, v23\n" \
  "v_exp_f32 v24, v24\nv_exp_f32 v25, v25\nv_smfmac_f32_32x32x32_bf16 %[OB], v[64:67], v[16:23], %[idx]\nds_read_b64_tr_b16 v[64:65], %[aV] offset:37888\n" \
  "ds_read_b64_tr_b16 v[66:67], %[aV2] offset:37888\nv_exp_f32 v26, v26\nv_exp_f32 v27, v27\nv_exp_f32 v28, v28\nv_exp_f32 v29, v29\n" \
  "v_mfma_f32_32x32x8_bf16 v[48:63], v[78:79], %[QB1], v[48:63]\nv_exp_f32 v30, v30\nv_exp_f32 v31, v31\ns_setprio 0\nv_exp_f32 v32, v32\nv_exp_f32 v33, v33\n" \
  "v_smfmac_f32_32x32x32_bf16 %[OB], v[68:71], v[24:31], %[idx]\nds_read_b64_tr_b16 v[68:69], %[aV] offset:38656\nds_read_b64_tr_b16 v[70:71], %[aV2] offset:38656\n" \
  "v_exp_f32 v34, v34\nv_exp_f32 v35, v35\nv_exp_f32 v36, v36\nv_exp_f32 v37, v37\ns_waitcnt lgkmcnt(2)\nv_exp_f32 v38, v38\nv_exp_f32 v39, v39\nv_exp_f32 v40, v40\n" \
  "v_exp_f32 v41, v41\nv_smfmac_f32_32x32x32_bf16 %[OA], v[64:67], v[32:39], %[idx]\nv_exp_f32 v42, v42\nv_exp_f32 v43, v43\nv_exp_f32 v44, v44\nv_exp_f32 v45, v45\n" \
  "v_exp_f32 v46, v46\nv_exp_f32 v47, v47\nv_exp_f32 v48, v48\nv_exp_f32 v49, v49\ns_waitcnt lgkmcnt(0)\nv_smfmac_f32_32x32x32_bf16 %[OA], v[68:71], v[40:47], %[idx]\n" \
  "v_exp_f32 v50, v50\nv_exp_f32 v51, v51\nv_exp_f32 v52, v52\nv_exp_f32 v53, v53\nv_exp_f32 v54, v54\nv_exp_f32 v55, v55\nv_exp_f32 v56, v56\nv_exp_f32 v57, v57\n" \
  "v_smfmac_f32_32x32x32_bf16 %[OB], v[64:67], v[48:55], %[idx]\nv_exp_f32 v58, v58\nv_exp_f32 v59, v59\nv_exp_f32 v60, v60\nv_exp_f32 v61, v61\nv_exp_f32 v62, v62\n" \
  "v_exp_f32 v63, v63\ns_setprio 0\ns_nop 0\nv_smfmac_f32_32x32x32_bf16 %[OB], v[68:71], v[56:63], %[idx]\ns_waitcnt vmcnt(0) lgkmcnt(0)\ns_nop 15\n"

DI f32x16 qk(u32x4 Km, u32x2 Kr, u32x4 Qm, u32x2 Qr, f32x16 c) {
  c = __builtin_amdgcn_mfma_f32_32x32x16_bf16(__builtin_bit_cast(bf16x8, Km), __builtin_bit_cast(bf16x8, Qm), c, 0, 0, 0);
  return __builtin_amdgcn_mfma_f32_32x32x8bf16_1k(__builtin_bit_cast(s16x4, Kr), __builtin_bit_cast(s16x4, Qr), c, 0, 0, 0);
}
DI f32x16 splat(float v) { f32x16 r;
#pragma unroll
  for (int i = 0; i < 16; i++) r[i] = v;
  return r; }
DI float max16(f32x16 s) { float m = s[0];
#pragma unroll
  for (int i = 1; i < 16; i++) m = fmaxf(m, s[i]);
  return m; }
DI float other_half(float v) {
  const auto r = __builtin_amdgcn_permlane32_swap(__float_as_uint(v), __float_as_uint(v), false, false);
  return __uint_as_float((threadIdx.x & 32) ? r[0] : r[1]);
}
DI f32x16 attn_exact(rsrc_t ws, unsigned kvb, u32x4 Qm, u32x2 Qr0, int lane) {
  const int h = lane >> 5, r = lane & 31;
  float m = -INFINITY;
  f32x16 O = splat(0.f);
#pragma unroll 1
  for (int ps = 0; ps < 2; ps++) {
#pragma unroll 1
    for (int t = 0; t < 64; t++) {
      const unsigned pb = kvb + (t >> 1) * PAIR;
      const f32x16 s = qk(ld16(ws, pb + (t & 1) * 1024 + lane * 16), ld8(ws, pb + 2048 + (t & 1) * 256 + r * 8), Qm, Qr0, splat(ps ? -m : 0.f));
      if (!ps) { m = fmaxf(m, max16(s)); continue; }
#pragma unroll 1
      for (int hf = 0; hf < 2; hf++) {
        f32x8 pe; u32x4 V;
#pragma unroll
        for (int i = 0; i < 8; i++) pe[i] = __builtin_amdgcn_exp2f(hf ? s[8 + i] : s[i]);
#pragma unroll
        for (int i = 0; i < 8; i++) {
          const unsigned key = 8 * h + i;
          const unsigned o = pb + 2560 + (t & 1) * 1536 + hf * 768 + v_off(min(r >> 3, 2), key) + (r < 24 ? r & 7 : 7) * 2;
          const unsigned x = __builtin_amdgcn_raw_buffer_load_b16(ws, o, 0, SC1);
          if (i & 1) V[i >> 1] |= x << 16; else V[i >> 1] = x;
        }
        O = __builtin_amdgcn_smfmac_f32_32x32x32_bf16(__builtin_bit_cast(bf16x8, V), __builtin_bit_cast(bf16x16, pe), O, 0xdddd, 0, 0);
      }
    }
    m = fmaxf(m, other_half(m));
  }
  return O;
}

__global__ void __launch_bounds__(512, 2) fused(P p) {
  const int b = blockIdx.x, xcd = b & 7, bi = b >> 3, tid = threadIdx.x;
  const int wave = __builtin_amdgcn_readfirstlane(tid >> 6), lane = tid & 63;
  const rsrc_t ws = __builtin_amdgcn_make_buffer_rsrc(p.ws, 0, WS_END, 0x00020000);
  const int rj = xcd * 4 + (bi >> 3), cj = bi & 7;
  const int odd = b & 1, ch0 = 5 * (b >> 1) + 2 * odd;
  STAMP(0);
  const int rb = (xcd >> 1) * 4 + (bi >> 3), cg = (xcd & 1) * 8 + (bi & 7);
  Pre pre; u32x2 tag;
  if (wave < 4) {
    gemm_phase(p, ws, rb, cg, rj, cj, ch0, wave, lane, pre, tag);
    STAMP(6);
  } else {
    if (wave == 7) { loader(p, ws, rb, cg, rj, cj, ch0, lane); precompute(pre); tag = call_tag(); } else kgen(p, wave - 4, lane, pre, tag);
    STAMP(7);
  }
  bar();
  windows(pre);
  WAITV(0);
  bar();
  if (tid == 448 && cg < 15) st8(ws, F_Q + (cg * 16 + rb) * 8, tag);
  const int c0 = (wave >> 2) + odd, c1 = odd ? 0 : 2;
  STAMP(8);
  const int head = rj, chunk = cj;
  const char* kvg = p.ws + WS_KV + head * 16 * POS;
  const int qg = wave & 3, kh = wave >> 2, h = lane >> 5, q = lane & 31, ts = wave & 1;
  const unsigned l16 = lane * 16, ra = L_RED + l16;
  f32x4 acc[4], y0, y1, y2;
  auto both = [&](u32x4 f) { return __builtin_amdgcn_ballot_w64(f[0] != tag[0] || f[1] != tag[1] || f[2] != tag[0] || f[3] != tag[1]) == 0; };
  bool staged = false;
  u32x4 QmA, QmB; u32x2 qa, qb;
  const unsigned qA = WS_Q + (head * 64 + chunk * 8 + qg * 2 + kh) * QT, qB = WS_Q + (head * 64 + chunk * 8 + qg * 2 + (kh ^ 1)) * QT;
  asm volatile("" : "={v[240:243]}"(QmA), "={v[244:247]}"(QmB), "={v[248:249]}"(qa), "={v[250:251]}"(qb));
  auto stage = [&]() {
        asm volatile("buffer_load_dwordx4 %0, %4, %6, %7 offen sc1\n buffer_load_dwordx4 %1, %4, %6, %8 offen sc1\n"
                     "buffer_load_dwordx2 %2, %5, %6, %7 offen offset:1024 sc1\n buffer_load_dwordx2 %3, %5, %6, %8 offen offset:1024 sc1"
                     : "+{v[240:243]}"(QmA), "+{v[244:247]}"(QmB), "+{v[248:249]}"(qa), "+{v[250:251]}"(qb) : "v"(l16), "v"(q * 8), "s"(ws), "s"(qA), "s"(qB));
        set_m0(wave * 7168 + 3072);
        const char* s0 = kvg + wave * 7168 + 3072;
        dma<-3072, true>(s0, l16); dma<-2048, true>(s0, l16); dma<-1024, true>(s0, l16); dma<0, true>(s0, l16); dma<1024, true>(s0, l16); dma<2048, true>(s0, l16); dma<3072, true>(s0, l16);
  };
#pragma unroll 1
  for (int rd = 0; rd < 2 - kh; rd++) {
    const int rq = rd ? wave : (wave >> 1) & 1;
    const u32x4 pf = conv_mma(ws, pre, rd ? c1 : c0, rd ? odd : ts, rd ? 2 * rq : 4 * rq, 2 - rd, acc);
    STAMP(9);
    if (!staged) {
      staged = both(pf);
      if (!staged && wave == 7) {
#pragma unroll 1
        for (int i = 0; i < (1 << 16); i++) { u32x4 f = ld16(ws, pre.fq); asm volatile("" : "+v"(f) :: "memory"); if (both(f)) break; __builtin_amdgcn_s_sleep(POLL_SLEEP); }
        staged = true;
      }
      if (staged) stage();
    }
    if (rd == 0) {
      if (rq) { ldsw<f32x4>(ra + wave * 2048, acc[0]); ldsw<f32x4>(ra + wave * 2048 + 1024, acc[1]); y0 = acc[2]; y1 = acc[3]; }
      else { ldsw<f32x4>(ra + wave * 2048, acc[2]); ldsw<f32x4>(ra + wave * 2048 + 1024, acc[3]); y0 = acc[0]; y1 = acc[1]; }
    } else {
#pragma unroll
      for (int o = 0; o < 4; o++) ldsw<f32x4>(ra + wave * 1024 + 16384 + o * 4096, acc[o]);
    }
  }
  STAMP(26);
  bar();
  if (!staged) stage();
  STAMP(10);
  const int sh0 = ldsr<int>(L_MISC + 32 + c0 * 4) - ldsr<int>(L_MISC + 16 + c0 * 4), sh1 = ldsr<int>(L_MISC + 32 + c1 * 4) - ldsr<int>(L_MISC + 16 + c1 * 4);
  y0 += ldsr<f32x4>(ra + (wave ^ 2) * 2048); y1 += ldsr<f32x4>(ra + (wave ^ 2) * 2048 + 1024);
  { const unsigned r1 = ra + 16384 + qg * 4096;
    y2 = (ldsr<f32x4>(r1) + ldsr<f32x4>(r1 + 1024)) + (ldsr<f32x4>(r1 + 2048) + ldsr<f32x4>(r1 + 3072)); }
  auto ypack = [&](f32x4 y, int sh) { return u32x2{pk_bf16(ldexpf(y[0], sh), ldexpf(y[1], sh)), pk_bf16(ldexpf(y[2], sh), ldexpf(y[3], sh))}; };
  u32x2 yp0 = ypack(y0, sh0), yp1 = ypack(y1, sh0), yp2 = ypack(y2, sh1);
  STAMP(11);
  unsigned aK = kh * PAIR + l16, aR = kh * PAIR + 2048 + lane * 8, aV = pre.aV, aV2 = pre.aV2;
  f32x16 NA, NB, OA, OB, S0, S1, S2, S3;
  u32x4 V0, V1 = {0, 0, 0, 0}, Km; u32x2 KrE, KrO;
  asm volatile("s_nop 1\n v_mfma_f32_32x32x8_bf16 %0, v[68:69], v[68:69], 0\n v_mfma_f32_32x32x8_bf16 %1, v[68:69], v[68:69], 0\n v_mfma_f32_32x32x8_bf16 %2, v[68:69], v[68:69], 0"
               : "=v"(OA), "=v"(OB), "={v[48:63]}"(S3), "+{v[68:71]}"(V1));
  asm volatile("s_waitcnt vmcnt(0)" : "+{v[240:243]}"(QmA), "+{v[244:247]}"(QmB), "+{v[248:249]}"(qa), "+{v[250:251]}"(qb), "+v"(yp0), "+v"(yp1), "+v"(yp2) :: "memory");
  {
    const unsigned ylo = pre.ylo, j0 = wave & 2;
    st8s(ws, ylo, WS_YT + (ch0 + c0) * 4096 + 512 * (2 * j0 + ts), yp0);
    st8s(ws, ylo, WS_YT + (ch0 + c0) * 4096 + 512 * (2 * j0 + 3 - ts), yp1);
    if (!kh) st8s(ws, ylo, WS_YT + (ch0 + c1) * 4096 + 512 * (2 * wave + ((wave & 1) ^ odd)), yp2);
  }
  bar();
  STAMP(12);
  const u32x2 z2 = {0, 0};
  const u32x2 QA0 = h ? z2 : qa, QA1 = h ? qa : z2, QB0 = h ? z2 : qb, QB1 = h ? qb : z2;
  {
    const u32x4 k0 = ldsr<u32x4>(l16); const u32x2 r0 = ldsr<u32x2>(2048 + lane * 8);
    Km = ldsr<u32x4>(aK); KrE = ldsr<u32x2>(aR);
    const float ma = max16(qk(k0, r0, QmA, QA0, splat(0.f))), mb = max16(qk(k0, r0, QmB, QB0, splat(0.f)));
    const auto sw = __builtin_amdgcn_permlane32_swap(__float_as_uint(ma), __float_as_uint(mb), false, false);
    const unsigned m2 = pk_bf16(-fmaxf(__uint_as_float(sw[0]), __uint_as_float(sw[1])), 0.f);
    const u32x2 mm = {m2, 0}, oA = {h ? 0u : 0x3f80u, 0}, oB = {h ? 0x3f80u : 0u, 0};
    NA = __builtin_amdgcn_mfma_f32_32x32x8bf16_1k(__builtin_bit_cast(s16x4, oA), __builtin_bit_cast(s16x4, mm), splat(0.f), 0, 0, 0);
    NB = __builtin_amdgcn_mfma_f32_32x32x8bf16_1k(__builtin_bit_cast(s16x4, oB), __builtin_bit_cast(s16x4, mm), splat(0.f), 0, 0, 0);
    S0 = qk(Km, KrE, QmA, QA0, NA); S1 = qk(Km, KrE, QmB, QB0, NB);
    Km = ldsr<u32x4>(aK + 1024);
    V0 = cat(ldtr(aV + 2560), ldtr(aV2 + 2560));
  }
  const unsigned idx = 0xddddu;
  const unsigned fyo = F_Y + cj * 256 + q * 8;
  constexpr unsigned L_Y = L_WO + 102400, L_Y2 = 24576;
  const char* ysrc = p.ws + WS_YT + cj * 80 * 4096 + rj * 128;
  const int yp2nd = wave < 2 ? 8 + wave : wave;
  STAMP(13);
  asm volatile(ATTN_ASM
               : [OA] "+v"(OA), [OB] "+v"(OB), "+{v[0:15]}"(S0), "+{v[16:31]}"(S1), "=&{v[32:47]}"(S2), "+{v[48:63]}"(S3),
                 "+{v[64:67]}"(V0), "+{v[68:71]}"(V1), "+{v[72:75]}"(Km), "+{v[76:77]}"(KrE), "=&{v[78:79]}"(KrO),
                 [aK] "+v"(aK), [aR] "+v"(aR), [aV] "+v"(aV), [aV2] "+v"(aV2)
               : [NA] "v"(NA), [NB] "v"(NB), [QmA] "{v[240:243]}"(QmA), [QmB] "{v[244:247]}"(QmB), [QA0] "v"(QA0), [QA1] "v"(QA1), [QB0] "v"(QB0), [QB1] "v"(QB1),
                 [idx] "v"(idx), "{v80}"(l16), "{v[88:95]}"(pre.vw), [src] "s"(kvg + 5 * POS + wave * 6144 + 3072), [dst] "s"(5 * POS + wave * 6144 + 3072), [wv] "s"(wave),
                 [wsrc] "s"(p.wo + cj * 160 + wave * 32 * 1280), [wdst] "s"(L_WO + wave * 5120 + 3072), [tag] "v"(tag), [ws] "s"(ws), [fym] "s"(F_Y + b * 8), [fyo] "v"(fyo),
                 [ys0] "s"(ysrc + wave * 32768), [ys1] "s"(ysrc + yp2nd * 32768), [yd0] "s"(L_Y + wave * 1024), [yd1] "s"(L_Y + yp2nd * 1024)
               : "memory", "scc", "vcc", "v96", "v97", "s40", "s41", "s48");
  STAMP(14);
  const int g = lane >> 4, tk = lane & 15;
  const rsrc_t ur = __builtin_amdgcn_make_buffer_rsrc((void*)p.u, 0, 2048 * 1280, 0x00020000), dr = __builtin_amdgcn_make_buffer_rsrc((void*)p.dsk, 0, 1280, 0x00020000);
  const rsrc_t outr = __builtin_amdgcn_make_buffer_rsrc(p.out, 0, 2048 * 2560, 0x00020000);
  unsigned row[2], oc16[2];
  u32x4 uu[2], dd[2];
#pragma unroll
  for (int i = 0; i < 2; i++) {
    row[i] = ((tid + 512 * i) * 6554) >> 16; oc16[i] = (tid + 512 * i) * 16 - row[i] * 160;
    const unsigned off = (i && wave >= 2) ? 0x80000000u : 0;
    uu[i] = __builtin_amdgcn_raw_buffer_load_b128(ur, (row[i] * 1280 + oc16[i]) | off, rj * 64 * 1280 + cj * 160, 0);
    dd[i] = __builtin_amdgcn_raw_buffer_load_b128(dr, oc16[i] | off, cj * 160, 0);
  }
  bar();
#pragma unroll
  for (int i = 0; i < 3; i++) ldsw<f32x4>(wave * 3072 + i * 1024 + l16, f32x4{OB[4 * i], OB[4 * i + 1], OB[4 * i + 2], OB[4 * i + 3]});
  bar();
  {
    f32x16 O = OA;
#pragma unroll
    for (int i = 0; i < 3; i++) { const f32x4 t = ldsr<f32x4>((wave ^ 4) * 3072 + i * 1024 + l16);
#pragma unroll
      for (int j = 0; j < 4; j++) O[4 * i + j] += t[j]; }
    auto rowsum = [&]() { return __uint_as_float(__builtin_amdgcn_permlane32_swap(__float_as_uint(O[8]), __float_as_uint(O[8]), false, false)[1]); };
    float l = rowsum();
    f32x2 sq = {0.f, 0.f};
#pragma unroll
    for (int i = 0; i < 6; i++) { const f32x2 t = {O[2 * i], O[2 * i + 1]}; sq = __builtin_elementwise_fma(t, t, sq); }
    const bool ok = l >= 0x1p-60f && sq[0] + sq[1] < INFINITY;
    if (__builtin_amdgcn_ballot_w64(!ok) != 0) { O = attn_exact(ws, WS_KV + head * 16 * POS, QmA, QA0, lane); l = rowsum(); }
    const float inv = __builtin_amdgcn_rcpf(l);
    const unsigned qt = chunk * 8 + qg * 2 + kh;
    st16s(ws, l16, WS_O + (head * 64 + qt) * 1024, u32x4{pk_bf16(O[0] * inv, O[1] * inv), pk_bf16(O[2] * inv, O[3] * inv), pk_bf16(O[4] * inv, O[5] * inv), pk_bf16(O[6] * inv, O[7] * inv)});
    st8s(ws, h ? 0x80000000u : l16, WS_OT + (((head >> 3) * 4 + (head & 3)) * 64 + qt) * 512 + (head & 4) * 2, u32x2{pk_bf16(O[8] * inv, O[9] * inv), pk_bf16(O[10] * inv, O[11] * inv)});
  }
  STAMP(4);
  const int wm = wave & 3, wk = wave >> 2, qq = (lane >> 2) & 3, pp = lane & 3;
  const unsigned am = L_WO + ((g >> 1) * 20 + (g & 1) * 4 + qq) * 160 + pp * 8 + wk * 400 * 160;
  u32x4 A[6][5];
#pragma unroll
  for (int s = 0; s < 6; s++) {
#pragma unroll
    for (int c = 0; c < 5; c++) A[s][c] = cat(ldtr(am + s * 6400 + c * 32), ldtr(am + s * 6400 + 1280 + c * 32));
    asm volatile("" : "+v"(A[s][0]), "+v"(A[s][1]), "+v"(A[s][2]), "+v"(A[s][3]), "+v"(A[s][4]));
  }
#pragma unroll
  for (int i = 0; i < 2; i++) {
    if (i == 0 || wave < 2) {
      const unsigned a = L_Y + ((i ? 64 + wave * 8 : kh * 32 + g * 8) + qq) * 128 + (i ? g : qg) * 32 + pp * 8;
      ldsw<u32x4>(L_Y2 + (i ? lane * 176 + 128 + wave * 16 : (qg * 16 + tk) * 176 + kh * 64 + g * 16), cat(ldtr(a), ldtr(a + 4 * 128)));
    }
  }
  WAITV(0); bar();
  if (tid == 448) st8(ws, F_O + (chunk * 32 + head) * 8, tag);
  STAMP(15);
  if (b == 0 && wave == 6) {
    u32x2 f[4];
#pragma unroll
    for (int i = 0; i < 4; i++) f[i] = ld8(ws, F_U + (i * 64 + lane) * 8);
    bool all = true;
#pragma unroll
    for (int i = 0; i < 4; i++) all = all && f[i][0] == tag[0] && f[i][1] == tag[1];
    if (__builtin_amdgcn_ballot_w64(!all) != 0) {
#pragma unroll 1
      for (int i = 0; i < 4; i++) tags_wait(ws, F_U + (i * 64 + lane) * 8, tag);
    }
    if (lane == 0) st8(ws, F_E, tag);
  }
  const unsigned fo = F_O + ((rj >> 2) * 32 + q) * 8;
  const u32x2 f0 = ld8(ws, wave == 7 ? fo : 0x80000000u);
#pragma unroll
  for (int i = 0; i < 2; i++) {
    if (i == 0 || wave < 2) {
      const u32x4 y = ldsr<u32x4>(L_Y2 + row[i] * 176 + oc16[i]);
      u32x4 o;
#pragma unroll
      for (int j = 0; j < 4; j++) {
        const unsigned t = pk_bf16(__uint_as_float(uu[i][j] << 16) * __uint_as_float(dd[i][j] << 16), __uint_as_float(uu[i][j] & 0xffff0000u) * __uint_as_float(dd[i][j] & 0xffff0000u));
        o[j] = pk_bf16(__uint_as_float(y[j] << 16) + __uint_as_float(t << 16), __uint_as_float(y[j] & 0xffff0000u) + __uint_as_float(t & 0xffff0000u));
      }
      st16x<OUT_AUX_A>(outr, row[i] * 2560 + oc16[i], rj * 64 * 2560 + cj * 160, o);
    }
  }
  if (wave == 7 && __builtin_amdgcn_ballot_w64(f0[0] != tag[0] || f0[1] != tag[1]) != 0) tags_wait(ws, fo, tag);
  bar();
  STAMP(17);
  const unsigned ot = rj * 2 + (wm >> 1), mo = (g >> 1) * 65536 + (g & 1) * 512 + tk * 16, ob = WS_O + ot * 1024 + (wm & 1) * 256;
  const unsigned ro = wk ? g * 32768 + tk * 16 : mo, rb2 = wk ? WS_OT + ot * 512 + (wm & 1) * 256 : ob + 6 * 131072;
  u32x4 B[10];
#pragma unroll
  for (int s = 0; s < 10; s++) B[s] = s < 6 ? ld16s(ws, mo, ob + (wk * 10 + s) * 131072) : ld16s(ws, ro, rb2 + (s - 6) * 131072);
  unsigned a0 = wk ? L_WO + (g * 20 + 16 + qq) * 160 + pp * 8 : am + 6 * 6400, a1 = a0 + (wk ? 12800 : 1280);
  const unsigned as = wk ? 25600 : 6400;
  STAMP(16);
  {
    f32x4 oc[5];
#pragma unroll
    for (int c = 0; c < 5; c++) oc[c] = f32x4{0.f, 0.f, 0.f, 0.f};
#pragma unroll
    for (int s = 0; s < 10; s++) {
#pragma unroll
      for (int c = 0; c < 5; c++) oc[c] = __builtin_amdgcn_mfma_f32_16x16x32_bf16(__builtin_bit_cast(bf16x8, A[s % 6][c]), __builtin_bit_cast(bf16x8, B[s]), oc[c], 0, 0, 0);
      if (s < 4) {
#pragma unroll
        for (int c = 0; c < 5; c++) A[s][c] = cat(ldtr(a0 + c * 32), ldtr(a1 + c * 32));
        a0 += as; a1 += as;
      }
    }
    STAMP(18);
    constexpr unsigned L_X = 0, L_X2 = 64 * 336;
#pragma unroll
    for (int c = 0; c < 5; c++) ldsw<f32x4>((wk ? L_X2 : L_X) + (wm * 16 + tk) * 336 + g * 16 + c * 64, oc[c]);
    bar();
#pragma unroll
    for (int i = 0; i < 2; i++) {
      if (i == 0 || wave < 2) {
        const unsigned a = row[i] * 336 + oc16[i] * 2;
        const f32x4 t0 = ldsr<f32x4>(L_X + a) + ldsr<f32x4>(L_X2 + a), t1 = ldsr<f32x4>(L_X + a + 16) + ldsr<f32x4>(L_X2 + a + 16);
        st16x<OUT_AUX_B>(outr, row[i] * 2560 + oc16[i], rj * 64 * 2560 + 1280 + cj * 160, u32x4{pk_bf16(t0[0], t0[1]), pk_bf16(t0[2], t0[3]), pk_bf16(t1[0], t1[1]), pk_bf16(t1[2], t1[3])});
      }
    }
  }
  STAMP(19);
  asm volatile(".Lfused_last:\n s_nop 0");
}

extern "C" void kernel_launch(void* const* d_in, const int* in_sizes, int n_in, void* d_out, int out_size, void* d_ws, size_t ws_size,
                              hipStream_t stream) {
  P p;
  p.u = (const char*)d_in[0]; p.lar = (const char*)d_in[1]; p.aim = (const char*)d_in[2]; p.cre = (const char*)d_in[3];
  p.cim = (const char*)d_in[4]; p.ldt = (const char*)d_in[5]; p.dsk = (const char*)d_in[6]; p.x = (const char*)d_in[7];
  p.wqkv = (const char*)d_in[8]; p.wo = (const char*)d_in[9]; p.out = (char*)d_out;
  p.ws = (char*)d_ws;
  (void)hipFuncSetAttribute((const void*)fused, hipFuncAttributeMaxDynamicSharedMemorySize, LDS_BYTES);
  fused<<<256, 512, LDS_BYTES, stream>>>(p);
}
```
